```python
import torch

def _softmax(x, axis=-1):
    return torch.softmax(x, dim=axis)
def _gelu(x):
    return torch.nn.functional.gelu(x, approximate="tanh")
def _silu(x):
    return torch.nn.functional.silu(x)
def _sigmoid(x):
    return torch.sigmoid(x)
def _layernorm(x, axis=-1, eps=1e-5):
    m = x.mean(dim=axis, keepdim=True)
    v = x.var(dim=axis, keepdim=True, unbiased=False)
    return (x - m) / torch.sqrt(v + eps)
def _scatter_add(tgt, idx, src, axis=0):
    return tgt.clone().index_add_(axis, idx, src)
def _segment_sum(src, seg, n):
    out = torch.zeros((n,) + src.shape[1:], dtype=src.dtype)
    return out.index_add_(0, seg, src)
def _pad(x, pw):
    flat = tuple(v for pair in reversed(pw) for v in pair)
    return torch.nn.functional.pad(x, flat)

kernel_name = 'sequential_preLN__sliding_local_dilated__dense_activation_silu__prompt'

def setup_inputs():

    _w = (lambda a: (lambda t: t.to(torch.bfloat16) if t.is_floating_point() else t)(torch.as_tensor(a)))
    def _A_setup():
        B, S, D = 1, 2048, 512
        return {
            "x": _w((lambda *s: torch.randn(*s, dtype=torch.bfloat16) * 0.05)(B, S, D)),
            "wqkv": _w((lambda *s: torch.randn(*s, dtype=torch.bfloat16) * 0.05)(D, 3 * D)),
            "wo": _w((lambda *s: torch.randn(*s, dtype=torch.bfloat16) * 0.05)(D, D)),
        }
    _A_in = _A_setup()

    def _B_setup():
        B, S, D, F = 1, 2048, 512, 1792
        return {
            "x": _w((lambda *s: torch.randn(*s, dtype=torch.bfloat16) * 0.05)(B, S, D)),
            "w1": _w((lambda *s: torch.randn(*s, dtype=torch.bfloat16) * 0.8)(D, F) * (D ** -0.5)),
            "w2": _w((lambda *s: torch.randn(*s, dtype=torch.bfloat16) * 0.8)(F, D) * (F ** -0.5)),
        }
    _B_in = _B_setup()

    return {
        "A_x": _A_in["x"],
        "A_wqkv": _A_in["wqkv"],
        "A_wo": _A_in["wo"],
        "B_x": _B_in["x"],
        "B_w1": _B_in["w1"],
        "B_w2": _B_in["w2"],
    }

def reference(A_x, A_wqkv, A_wo, B_x, B_w1, B_w2):
    def _A_ref(x, wqkv, wo):
        B, S, D = x.shape
        H = 32; Dh = D // H
        W, dil = 8, 2
        qkv = (lambda x, *d: x.permute(*d))((x @ wqkv).reshape(B, S, 3, H, Dh), 2, 0, 3, 1, 4)
        q, k, v = qkv[0], qkv[1], qkv[2]
        scale = Dh ** -0.5
        pos = torch.arange(S)
        off = torch.arange(W) * dil
        idx = pos[:, None] - off[None, :]
        mask = idx >= 0
        idx_c = torch.where(mask, idx, 0).reshape(-1)
        kg = k[:, :, idx_c, :].reshape(B, H, S, W, Dh)
        vg = v[:, :, idx_c, :].reshape(B, H, S, W, Dh)
        logits = torch.einsum("bhsd,bhswd->bhsw", q, kg) * scale
        logits = torch.where(mask[None, None, :, :], logits, -1e9)
        attn = _softmax(logits, axis=-1)
        o = torch.einsum("bhsw,bhswd->bhsd", attn, vg)
        return (lambda x, *d: x.permute(*d))(o, 0, 2, 1, 3).reshape(B, S, D) @ wo

    def _B_ref(x, w1, w2):
        h = torch.einsum("bsd,df->bsf", x, w1)
        h = _silu(h)
        out = torch.einsum("bsf,fd->bsd", h, w2)
        return out

    x = A_x
    h = x + _A_ref(_layernorm(x, axis=-1), A_wqkv, A_wo)
    h2 = h + _B_ref(_layernorm(h, axis=-1), B_w1, B_w2)
    return h2
```

```cpp
#include <hip/hip_runtime.h>
#include <hip/hip_bf16.h>
#include <stdint.h>

namespace {

typedef unsigned int u32;
typedef unsigned long long u64;
typedef unsigned short u16;
typedef u32 v4u __attribute__((ext_vector_type(4)));
typedef u32 v2u __attribute__((ext_vector_type(2)));
typedef float f32x4 __attribute__((ext_vector_type(4)));
typedef short s16x4 __attribute__((ext_vector_type(4)));
typedef short s16x8 __attribute__((ext_vector_type(8)));
typedef __bf16 bf2 __attribute__((ext_vector_type(2)));
typedef __bf16 bf8 __attribute__((ext_vector_type(8)));
#define GAS __attribute__((address_space(1)))
#define LAS __attribute__((address_space(3)))
#define RLX_AGENT __ATOMIC_RELAXED, __HIP_MEMORY_SCOPE_AGENT
typedef __amdgpu_buffer_rsrc_t rsrc_t;

constexpr int S_ = 2048, D_ = 512, F_ = 1792;
constexpr int NTHR = 512;
constexpr int AUX_SC1 = 16;
constexpr int RSRC_W3 = 0x00020000;
constexpr float EPS = 1e-5f;

constexpr int WS_FLAG = 0;
constexpr int WS_EPOCH = 3 * 128 * 128;
constexpr int WS_O = 65536;
constexpr int WS_H = WS_O + 128 * 16 * 1024;
constexpr int WS_F = WS_H + 128 * 16 * 1024;
constexpr int WS_END = WS_F + 128 * 56 * 1024;

constexpr int L_QKV = 0;
constexpr int L_HALO = 98304;
constexpr int L_W1 = 0;
constexpr int L_WO = 79872;
constexpr int L_S = 145408;
constexpr int L_SK = L_S;
constexpr int L_SV = L_S + 8192;
constexpr int L_CS = L_S + 8192 + 9216;
constexpr int L_RED = L_S;
constexpr int L_W2 = 0;
constexpr int W2_RES = 17;
constexpr int LDS_BYTES = 163840;
static_assert(L_CS + 448 <= LDS_BYTES, "lds");
static_assert(2 * W2_RES * 4096 <= L_S, "lds");

constexpr int SPIN_CAP = 1 << 17;

__device__ __forceinline__ void dma16(u32 lds, u32 voff, rsrc_t rs, u32 soff) {
  asm volatile("s_mov_b32 m0, %0\n\ts_nop 0\n\tbuffer_load_dwordx4 %1, %2, %3 offen lds"
               :: "s"(lds), "v"(voff), "s"(rs), "s"(soff) : "memory");
}
#define WAIT_VM(n) asm volatile("s_waitcnt vmcnt(" #n ")" ::: "memory")
#define WAIT_LGKM() asm volatile("s_waitcnt lgkmcnt(0)" ::: "memory")
#define BARRIER() do { asm volatile("s_waitcnt lgkmcnt(0)" ::: "memory"); __builtin_amdgcn_s_barrier(); asm volatile("" ::: "memory"); } while (0)

__device__ __forceinline__ s16x4 tr4(u32 a) {
  return __builtin_amdgcn_ds_read_tr16_b64_v4i16((LAS s16x4*)(uintptr_t)a);
}
__device__ __forceinline__ s16x8 cat8(s16x4 a, s16x4 b) { return __builtin_shufflevector(a, b, 0, 1, 2, 3, 4, 5, 6, 7); }
__device__ __forceinline__ f32x4 mfma32(s16x8 a, s16x8 b, f32x4 c) {
  return __builtin_amdgcn_mfma_f32_16x16x32_bf16(__builtin_bit_cast(bf8, a), __builtin_bit_cast(bf8, b), c, 0, 0, 0);
}
__device__ __forceinline__ f32x4 mfma32u(s16x8 a, v4u b, f32x4 c) { return mfma32(a, __builtin_bit_cast(s16x8, b), c); }
__device__ __forceinline__ f32x4 mfma16(v2u a, v2u b, f32x4 c) {
  return __builtin_amdgcn_mfma_f32_16x16x16bf16_1k(__builtin_bit_cast(s16x4, a), __builtin_bit_cast(s16x4, b), c, 0, 0, 0);
}
__device__ __forceinline__ u32 pk2(float lo, float hi) {
  bf2 p; p.x = (__bf16)lo; p.y = (__bf16)hi;
  return __builtin_bit_cast(u32, p);
}
__device__ __forceinline__ float bflo(u32 w) { return __builtin_bit_cast(float, w << 16); }
__device__ __forceinline__ float bfhi(u32 w) { return __builtin_bit_cast(float, w & 0xffff0000u); }
__device__ __forceinline__ void stat4(v4u a, float& s, float& q) {
  const bf2 one = __builtin_bit_cast(bf2, 0x3f803f80u);
#pragma unroll
  for (int r = 0; r < 4; ++r) {
    const u32 ar = a[r];
    const bf2 v = __builtin_bit_cast(bf2, ar);
    s = __builtin_amdgcn_fdot2_f32_bf16(v, one, s, false);
    q = __builtin_amdgcn_fdot2_f32_bf16(v, v, q, false);
  }
}
__device__ __forceinline__ float red_g(float v) {
  v += __shfl_xor(v, 16);
  v += __shfl_xor(v, 32);
  return v;
}
__device__ __forceinline__ void ln_stats(float s, float q, float& rstd, float& mr) {
  s = red_g(s); q = red_g(q);
  const float mean = s * (1.0f / D_);
  const float var = fmaxf(q * (1.0f / D_) - mean * mean, 0.0f);
  rstd = __builtin_amdgcn_rsqf(var + EPS);
  mr = mean * rstd;
}
__device__ __forceinline__ void wait_flags(const GAS u64* f, u64 tag, bool& dead) {
  if (!dead) {
    int spin = 0;
    for (;;) {
      const u64 v = __hip_atomic_load(f, RLX_AGENT);
      if (__all(v == tag)) break;
      if (++spin > SPIN_CAP) { dead = true; break; }
      __builtin_amdgcn_s_sleep(1);
    }
  }
  __builtin_amdgcn_fence(__ATOMIC_ACQUIRE, "wavefront");
}

#define STAMP(k) do { } while (0)

__global__ void __launch_bounds__(NTHR, 2)
fused_block(const u16* __restrict__ x, const u16* __restrict__ wqkv, const u16* __restrict__ wo,
            const u16* __restrict__ w1, const u16* __restrict__ w2, u16* __restrict__ out, unsigned char* ws) {
  extern __shared__ __attribute__((aligned(16))) unsigned char smem[];
  const u32 L0 = (u32)(uintptr_t)(LAS unsigned char*)smem;
  STAMP(0);
  const int tid = threadIdx.x, lane = tid & 63, li = lane & 15, g = lane >> 4;
  const int w = __builtin_amdgcn_readfirstlane(tid >> 6);
  const int b = blockIdx.x;
  const int p = (b & 7) * 2 + (b >> 7);
  const int j = (b >> 3) & 15;
  const int rh = j >> 3, cb = j & 7;
  const int rt = w & 3, kh = w >> 2;
  const int RT = 8 * p + w;
  const int RT2 = 8 * p + 4 * rh + rt;

  const rsrc_t rsX = __builtin_amdgcn_make_buffer_rsrc((void*)x, 0, S_ * D_ * 2, RSRC_W3);
  const rsrc_t rsQ = __builtin_amdgcn_make_buffer_rsrc((void*)wqkv, 0, D_ * 3 * D_ * 2, RSRC_W3);
  const rsrc_t rsO = __builtin_amdgcn_make_buffer_rsrc((void*)wo, 0, D_ * D_ * 2, RSRC_W3);
  const rsrc_t rs1 = __builtin_amdgcn_make_buffer_rsrc((void*)w1, 0, D_ * F_ * 2, RSRC_W3);
  const rsrc_t rs2 = __builtin_amdgcn_make_buffer_rsrc((void*)w2, 0, F_ * D_ * 2, RSRC_W3);
  const rsrc_t rsW = __builtin_amdgcn_make_buffer_rsrc((void*)ws, 0, WS_END, RSRC_W3);
  const rsrc_t rsOut = __builtin_amdgcn_make_buffer_rsrc((void*)out, 0, S_ * D_ * 2, RSRC_W3);
  GAS u64* const flags = (GAS u64*)(ws + WS_FLAG);
  GAS u64* const epoch = (GAS u64*)(ws + WS_EPOCH) + 16 * p;
  bool dead = false;

  const u64 tag = __hip_atomic_load(epoch, RLX_AGENT) + 1ull;
  {
    u32 voff[3];
#pragma unroll
    for (int bb = 0; bb < 3; ++bb) {
      const int chi = 64 * bb + lane, r = chi / 12, u = chi % 12;
      const int c = (u >> 1) ^ ((r >> 2) & 1);
      voff[bb] = (u32)((8 * (r >> 2) + (r & 3)) * 3072 + (512 * (c >> 1) + 32 * j + 16 * (c & 1) + 8 * (u & 1)) * 2);
    }
#pragma unroll
    for (int i = 0; i < 4; ++i) {
      const int a = w + 8 * i;
      const u32 so = (u32)((32 * (a >> 1) + 4 * (a & 1)) * 3072);
#pragma unroll
      for (int bb = 0; bb < 3; ++bb) dma16(L0 + L_QKV + (3 * a + bb) * 1024, voff[bb], rsQ, so);
    }
#pragma unroll
    for (int i = 0; i < 2; ++i) {
      const int r = 2 * w + i;
      const int row = max(128 * p - 16 + r, 0);
      if (r != 0) dma16(L0 + L_HALO + r * 1024, (u32)((lane ^ r) * 16), rsX, (u32)(row * 1024));
    }
    if (w == 0) *(LAS v4u*)(uintptr_t)(L0 + L_HALO + lane * 16) = v4u{0x3f803f80u, 0x3f803f80u, 0x3f803f80u, 0x3f803f80u};
  }
  __builtin_amdgcn_sched_barrier(0);
  v4u xa[16];
  {
    const u32 xo = (u32)((16 * RT + li) * 1024 + g * 16);
#pragma unroll
    for (int ks = 0; ks < 16; ++ks) xa[ks] = __builtin_amdgcn_raw_buffer_load_b128(rsX, xo + ks * 64, 0, 0);
  }
  v2u xr[2];
  {
    const u32 ro = (u32)((16 * RT2 + li) * 1024 + (64 * cb + 32 * kh + 4 * g) * 2);
    xr[0] = __builtin_amdgcn_raw_buffer_load_b64(rsX, ro, 0, 0);
    xr[1] = __builtin_amdgcn_raw_buffer_load_b64(rsX, ro + 32, 0, 0);
  }
  __builtin_amdgcn_sched_barrier(0);
  WAIT_VM(18);
  BARRIER();
  __builtin_amdgcn_sched_barrier(0);
  STAMP(1);

  const int q4 = (lane >> 2) & 3, p4 = lane & 3;
  const u32 qb0 = L0 + L_QKV + (4 * g + q4) * 192 + 8 * p4;
  const u32 qbe = qb0 + 32 * (g & 1), qbo = qb0 + 32 * ((g & 1) ^ 1);
  if (w < 6) {
    const u32 hb = ((w & 1) ? qbo : qbe) + 64 * (w >> 1);
    f32x4 ah = {0.f, 0.f, 0.f, 0.f};
    float s = 0.f, q = 0.f;
#pragma unroll
    for (int ks = 0; ks < 16; ++ks) {
      const v4u hx = *(LAS v4u*)(uintptr_t)(L0 + L_HALO + li * 1024 + (((4 * ks + g) ^ li) * 16));
      const s16x8 wf = cat8(tr4(hb + ks * 6144), tr4(hb + ks * 6144 + 3072));
      ah = mfma32u(wf, hx, ah);
      stat4(hx, s, q);
    }
    float rstd, mr;
    ln_stats(s, q, rstd, mr);
    f32x4 cs;
#pragma unroll
    for (int r = 0; r < 4; ++r) cs[r] = __shfl(ah[r], lane & 48);
    if (li == 0) *(LAS f32x4*)(uintptr_t)(L0 + L_CS + w * 64 + g * 16) = cs;
    v2u hv;
    hv.x = pk2(rstd * ah[0] - mr * cs[0], rstd * ah[1] - mr * cs[1]);
    hv.y = pk2(rstd * ah[2] - mr * cs[2], rstd * ah[3] - mr * cs[3]);
    if (w >= 4) *(LAS v2u*)(uintptr_t)(L0 + L_SV + (w & 1) * 512 + li * 32 + g * 8) = hv;
    else if (w >= 2) *(LAS v2u*)(uintptr_t)(L0 + L_SK + (w & 1) * 512 + lane * 8) = hv;
  }
  STAMP(2);
  f32x4 acc[7];
#pragma unroll
  for (int c = 0; c < 7; ++c) acc[c] = f32x4{0.f, 0.f, 0.f, 0.f};
  {
    float s = 0.f, q = 0.f;
#pragma unroll
    for (int ks = 0; ks < 16; ++ks) {
#pragma unroll
      for (int c = 0; c < 6; ++c) {
        const u32 a = ((c & 1) ? qbo : qbe) + 64 * (c >> 1) + ks * 6144;
        acc[c] = mfma32u(cat8(tr4(a), tr4(a + 3072)), xa[ks], acc[c]);
      }
      stat4(xa[ks], s, q);
      if (ks == 8) BARRIER();
    }
    STAMP(3);
    float rstd, mr;
    ln_stats(s, q, rstd, mr);
    v2u qkv[6];
#pragma unroll
    for (int c = 0; c < 6; ++c) {
      const f32x4 cs = *(LAS f32x4*)(uintptr_t)(L0 + L_CS + c * 64 + g * 16);
      qkv[c].x = pk2(rstd * acc[c][0] - mr * cs[0], rstd * acc[c][1] - mr * cs[1]);
      qkv[c].y = pk2(rstd * acc[c][2] - mr * cs[2], rstd * acc[c][3] - mr * cs[3]);
    }
#pragma unroll
    for (int hh = 0; hh < 2; ++hh) {
      if (w < 7) *(LAS v2u*)(uintptr_t)(L0 + L_SK + (w + 1) * 1024 + hh * 512 + lane * 8) = qkv[2 + hh];
      *(LAS v2u*)(uintptr_t)(L0 + L_SV + (w + 1) * 1024 + hh * 512 + li * 32 + g * 8) = qkv[4 + hh];
    }
    BARRIER();
    const int par = li & 1;
    const bool ownA = (4 * g + par) <= li, ownB = (4 * g + par + 2) <= li;
    const bool okA = ownA || RT > 0, okB = ownB || RT > 0;
    v4u of;
#pragma unroll
    for (int hh = 0; hh < 2; ++hh) {
      const v2u kp = *(LAS v2u*)(uintptr_t)(L0 + L_SK + w * 1024 + hh * 512 + lane * 8);
      const s16x8 vt = cat8(tr4(L0 + L_SV + w * 1024 + hh * 512 + lane * 8), tr4(L0 + L_SV + (w + 1) * 1024 + hh * 512 + lane * 8));
      const f32x4 z = {0.f, 0.f, 0.f, 0.f};
      const f32x4 s0 = mfma16(kp, qkv[hh], z), s1 = mfma16(qkv[2 + hh], qkv[hh], z);
      const float c2 = 0.25f * 1.44269504088896340736f;
      float sA = ownA ? (par ? s1[1] : s1[0]) : (par ? s0[1] : s0[0]);
      float sB = ownB ? (par ? s1[3] : s1[2]) : (par ? s0[3] : s0[2]);
      sA = okA ? sA * c2 : -1e30f;
      sB = okB ? sB * c2 : -1e30f;
      float m = fmaxf(sA, sB);
      m = fmaxf(m, __shfl_xor(m, 16));
      m = fmaxf(m, __shfl_xor(m, 32));
      const float eA = __builtin_amdgcn_exp2f(sA - m), eB = __builtin_amdgcn_exp2f(sB - m);
      const float inv = __builtin_amdgcn_rcpf(red_g(eA + eB));
      const u32 pw = pk2(eA * inv, eB * inv);
      const u32 wA = (pw & 0xffffu) << (16 * par), wB = (pw >> 16) << (16 * par);
      const v4u pf = {ownA ? 0u : wA, ownB ? 0u : wB, ownA ? wA : 0u, ownB ? wB : 0u};
      const f32x4 o = mfma32u(vt, pf, z);
      of[2 * hh] = pk2(o[0], o[1]);
      of[2 * hh + 1] = pk2(o[2], o[3]);
    }
    __builtin_amdgcn_raw_buffer_store_b128(of, rsW, (u32)(WS_O + (RT * 16 + j) * 1024 + lane * 16), 0, AUX_SC1);
  }
  STAMP(4);
  const u32 vo64 = (u32)((lane >> 3) * 1024 + (64 * cb + 16 * (((lane >> 1) & 3) ^ ((lane >> 4) & 3)) + 8 * (lane & 1)) * 2);
  u32 vo1;
  {
    const int chi = 64 * (w < 7 ? w : 0) + lane;
    vo1 = (u32)((chi / 14) * 3584 + (112 * j + 8 * (chi % 14)) * 2);
  }
#pragma unroll
  for (int i = 0; i < 8; ++i) dma16(L0 + L_WO + (w + 8 * i) * 1024, vo64, rsO, (u32)((w + 8 * i) * 8192));
  WAIT_VM(8);
  if (lane == 0) __hip_atomic_store(flags + (0 * 128 + RT) * 16 + j, tag, RLX_AGENT);
  if (w < 7) {
#pragma unroll
    for (int ks = 0; ks < 11; ++ks) dma16(L0 + L_W1 + (7 * ks + w) * 1024, vo1, rs1, (u32)(ks * 32 * 3584));
    WAIT_VM(11);
  } else {
    WAIT_VM(0);
  }
  BARRIER();
  STAMP(5);

  const u32 sw64 = (2 * g + (q4 >> 1)) & 3;
  u32 b64[4];
#pragma unroll
  for (int c = 0; c < 4; ++c) b64[c] = L0 + (4 * g + q4) * 128 + 32 * (c ^ sw64) + 8 * p4;
  v4u hfrag;
  {
    wait_flags(flags + (0 * 128 + RT2) * 16 + 8 * kh + (lane & 7), tag, dead);
    STAMP(6);
    v4u oa[8];
#pragma unroll
    for (int s = 0; s < 8; ++s)
      oa[s] = __builtin_amdgcn_raw_buffer_load_b128(rsW, (u32)(WS_O + (RT2 * 16 + 8 * kh + s) * 1024 + lane * 16), 0, AUX_SC1);
    __builtin_amdgcn_sched_barrier(0);
#pragma unroll
    for (int c = 0; c < 4; ++c) acc[c] = f32x4{0.f, 0.f, 0.f, 0.f};
#pragma unroll
    for (int s = 0; s < 8; ++s)
#pragma unroll
      for (int c = 0; c < 4; ++c) {
        const u32 a = b64[c] + L_WO + (8 * kh + s) * 4096;
        acc[c] = mfma32u(cat8(tr4(a), tr4(a + 2048)), oa[s], acc[c]);
      }
    STAMP(7);
    const f32x4 snd0 = kh ? acc[0] : acc[2], snd1 = kh ? acc[1] : acc[3];
    f32x4 k0 = kh ? acc[2] : acc[0], k1 = kh ? acc[3] : acc[1];
    *(LAS f32x4*)(uintptr_t)(L0 + L_RED + w * 2048 + lane * 16) = snd0;
    *(LAS f32x4*)(uintptr_t)(L0 + L_RED + w * 2048 + 1024 + lane * 16) = snd1;
    BARRIER();
    k0 += *(LAS f32x4*)(uintptr_t)(L0 + L_RED + (w ^ 4) * 2048 + lane * 16);
    k1 += *(LAS f32x4*)(uintptr_t)(L0 + L_RED + (w ^ 4) * 2048 + 1024 + lane * 16);
    const u32 a0 = pk2(k0[0], k0[1]), a1 = pk2(k0[2], k0[3]), a2 = pk2(k1[0], k1[1]), a3 = pk2(k1[2], k1[3]);
    hfrag.x = pk2(bflo(xr[0].x) + bflo(a0), bfhi(xr[0].x) + bfhi(a0));
    hfrag.y = pk2(bflo(xr[0].y) + bflo(a1), bfhi(xr[0].y) + bfhi(a1));
    hfrag.z = pk2(bflo(xr[1].x) + bflo(a2), bfhi(xr[1].x) + bfhi(a2));
    hfrag.w = pk2(bflo(xr[1].y) + bflo(a3), bfhi(xr[1].y) + bfhi(a3));
    __builtin_amdgcn_raw_buffer_store_b128(hfrag, rsW, (u32)(WS_H + (RT2 * 16 + 2 * cb + kh) * 1024 + lane * 16), 0, AUX_SC1);
  }
  if (w < 7) {
#pragma unroll
    for (int ks = 11; ks < 16; ++ks) dma16(L0 + L_W1 + (7 * ks + w) * 1024, vo1, rs1, (u32)(ks * 32 * 3584));
    WAIT_VM(5);
  } else {
    WAIT_VM(0);
  }
  if (lane == 0) __hip_atomic_store(flags + (1 * 128 + RT2) * 16 + 2 * cb + kh, tag, RLX_AGENT);
  STAMP(8);
  WAIT_VM(0);
  BARRIER();
  STAMP(9);

  {
    const u32 wb = L0 + L_W1 + (4 * g + q4) * 224 + 8 * p4;
    const int odd = j & 1;
    const u32 wbm = wb + 32 * odd, wbl = wb + (odd ? 0 : 192);
    if (w < 7) {
      const u32 a = (w < 6) ? wbm + 32 * w : wbl;
      const v4u ones = {0x3f803f80u, 0x3f803f80u, 0x3f803f80u, 0x3f803f80u};
      f32x4 cs = {0.f, 0.f, 0.f, 0.f};
#pragma unroll
      for (int ks = 0; ks < 16; ++ks) cs = mfma32u(cat8(tr4(a + ks * 7168), tr4(a + ks * 7168 + 3584)), ones, cs);
      if (li == 0) *(LAS f32x4*)(uintptr_t)(L0 + L_CS + w * 64 + g * 16) = cs;
    }
    wait_flags(flags + (1 * 128 + RT) * 16 + li, tag, dead);
    STAMP(10);
#pragma unroll
    for (int ks = 0; ks < 16; ++ks)
      xa[ks] = __builtin_amdgcn_raw_buffer_load_b128(rsW, (u32)(WS_H + (RT * 16 + ks) * 1024 + lane * 16), 0, AUX_SC1);
    __builtin_amdgcn_sched_barrier(0);
#pragma unroll
    for (int c = 0; c < 7; ++c) acc[c] = f32x4{0.f, 0.f, 0.f, 0.f};
    float s = 0.f, q = 0.f;
#pragma unroll
    for (int ks = 0; ks < 16; ++ks) {
#pragma unroll
      for (int c = 0; c < 7; ++c) {
        const u32 a = (c < 6 ? wbm + 32 * c : wbl) + ks * 7168;
        acc[c] = mfma32u(cat8(tr4(a), tr4(a + 3584)), xa[ks], acc[c]);
      }
      stat4(xa[ks], s, q);
    }
    STAMP(11);
    BARRIER();
    float rstd, mr;
    ln_stats(s, q, rstd, mr);
    u32 fw[14];
#pragma unroll
    for (int c = 0; c < 7; ++c) {
      const f32x4 cs = *(LAS f32x4*)(uintptr_t)(L0 + L_CS + c * 64 + g * 16);
      float y[4];
#pragma unroll
      for (int r = 0; r < 4; ++r) {
        const float t = rstd * acc[c][r] - mr * cs[r];
        y[r] = t * __builtin_amdgcn_rcpf(1.0f + __builtin_amdgcn_exp2f(t * -1.44269504088896340736f));
      }
      fw[2 * c] = pk2(y[0], y[1]);
      fw[2 * c + 1] = pk2(y[2], y[3]);
    }
    const u32 fo = (u32)(WS_F + (RT * 56 + 7 * (j >> 1)) * 1024 + lane * 16);
#pragma unroll
    for (int i = 0; i < 3; ++i)
      __builtin_amdgcn_raw_buffer_store_b128(v4u{fw[4 * i], fw[4 * i + 1], fw[4 * i + 2], fw[4 * i + 3]}, rsW,
                                             fo + (4 * odd + i) * 1024, 0, AUX_SC1);
    __builtin_amdgcn_raw_buffer_store_b64(v2u{fw[12], fw[13]}, rsW, fo + 3 * 1024 + 8 * odd, 0, AUX_SC1);
  }
  STAMP(12);
#pragma unroll
  for (int i = 0; i < W2_RES; ++i)
    dma16(L0 + L_W2 + (kh * W2_RES + i) * 4096 + rt * 1024, vo64, rs2, (u32)(((28 * kh + i) * 32 + 8 * rt) * 1024));
  WAIT_VM(17);
  if (lane == 0) __hip_atomic_store(flags + (2 * 128 + RT) * 16 + j, tag, RLX_AGENT);
  STAMP(13);
  WAIT_VM(0);
  BARRIER();
  STAMP(14);

  {
    wait_flags(flags + (2 * 128 + RT2) * 16 + 8 * kh + (lane & 7), tag, dead);
    STAMP(15);
    v4u fa[28];
#pragma unroll
    for (int s = 0; s < 28; ++s)
      fa[s] = __builtin_amdgcn_raw_buffer_load_b128(rsW, (u32)(WS_F + (RT2 * 56 + 28 * kh + s) * 1024 + lane * 16), 0, AUX_SC1);
    __builtin_amdgcn_sched_barrier(0);
#pragma unroll
    for (int c = 0; c < 4; ++c) acc[c] = f32x4{0.f, 0.f, 0.f, 0.f};
    u32 bb[4];
#pragma unroll
    for (int c = 0; c < 4; ++c) bb[c] = b64[c] + L_W2 + kh * W2_RES * 4096;
#pragma unroll
    for (int s = 0; s < 28; ++s) {
      if (s == 28 - W2_RES) {
        BARRIER();
#pragma unroll
        for (int i = 0; i < 28 - W2_RES; ++i)
          dma16(L0 + L_W2 + (kh * W2_RES + i) * 4096 + rt * 1024, vo64, rs2, (u32)(((28 * kh + W2_RES + i) * 32 + 8 * rt) * 1024));
      }
      if (s == W2_RES) {
        WAIT_VM(0);
        BARRIER();
      }
      const int slot = s < W2_RES ? s : s - W2_RES;
#pragma unroll
      for (int c = 0; c < 4; ++c) {
        const u32 a = bb[c] + slot * 4096;
        acc[c] = mfma32u(cat8(tr4(a), tr4(a + 2048)), fa[s], acc[c]);
      }
    }
    STAMP(16);
    const f32x4 snd0 = kh ? acc[0] : acc[2], snd1 = kh ? acc[1] : acc[3];
    f32x4 k0 = kh ? acc[2] : acc[0], k1 = kh ? acc[3] : acc[1];
    *(LAS f32x4*)(uintptr_t)(L0 + L_RED + w * 2048 + lane * 16) = snd0;
    *(LAS f32x4*)(uintptr_t)(L0 + L_RED + w * 2048 + 1024 + lane * 16) = snd1;
    BARRIER();
    k0 += *(LAS f32x4*)(uintptr_t)(L0 + L_RED + (w ^ 4) * 2048 + lane * 16);
    k1 += *(LAS f32x4*)(uintptr_t)(L0 + L_RED + (w ^ 4) * 2048 + 1024 + lane * 16);
    const u32 a0 = pk2(k0[0], k0[1]), a1 = pk2(k0[2], k0[3]), a2 = pk2(k1[0], k1[1]), a3 = pk2(k1[2], k1[3]);
    u32 o0 = pk2(bflo(hfrag.x) + bflo(a0), bfhi(hfrag.x) + bfhi(a0));
    u32 o1 = pk2(bflo(hfrag.y) + bflo(a1), bfhi(hfrag.y) + bfhi(a1));
    u32 o2 = pk2(bflo(hfrag.z) + bflo(a2), bfhi(hfrag.z) + bfhi(a2));
    u32 o3 = pk2(bflo(hfrag.w) + bflo(a3), bfhi(hfrag.w) + bfhi(a3));
    const auto s0 = __builtin_amdgcn_permlane16_swap(o0, o2, false, false);
    const auto s1 = __builtin_amdgcn_permlane16_swap(o1, o3, false, false);
    const v4u ov = {s0[0], s1[0], s0[1], s1[1]};
    const u32 oo = (u32)((16 * RT2 + li) * 1024 + (64 * cb + 32 * kh + 16 * (g & 1) + 8 * (g >> 1)) * 2);
    __builtin_amdgcn_raw_buffer_store_b128(ov, rsOut, oo, 0, 0);
    if (j == 0 && tid == 0) __hip_atomic_store(epoch, tag, RLX_AGENT);
  }
  STAMP(17);
}

}

extern "C" void kernel_launch(void* const* d_in, const int* in_sizes, int n_in, void* d_out, int out_size, void* d_ws,
                              size_t ws_size, hipStream_t stream) {
  (void)in_sizes; (void)n_in; (void)out_size; (void)ws_size;
  (void)hipFuncSetAttribute((const void*)fused_block, hipFuncAttributeMaxDynamicSharedMemorySize, LDS_BYTES);
  hipLaunchKernelGGL(fused_block, dim3(256), dim3(NTHR), LDS_BYTES, stream, (const u16*)d_in[0], (const u16*)d_in[1],
                     (const u16*)d_in[2], (const u16*)d_in[4], (const u16*)d_in[5], (u16*)d_out, (unsigned char*)d_ws);
}
```

```cpp
#include <hip/hip_runtime.h>
#include <hip/hip_bf16.h>
#include <stdint.h>
#include <type_traits>

namespace {

typedef unsigned int u32;
typedef unsigned long long u64;
typedef unsigned short u16;
typedef u32 v4u __attribute__((ext_vector_type(4)));
typedef u32 v2u __attribute__((ext_vector_type(2)));
typedef u32 v8u __attribute__((ext_vector_type(8)));
typedef float f32x4 __attribute__((ext_vector_type(4)));
typedef float f32x2 __attribute__((ext_vector_type(2)));
typedef short s16x4 __attribute__((ext_vector_type(4)));
typedef short s16x8 __attribute__((ext_vector_type(8)));
typedef __bf16 bf2 __attribute__((ext_vector_type(2)));
typedef __bf16 bf8 __attribute__((ext_vector_type(8)));
#define GAS __attribute__((address_space(1)))
#define LAS __attribute__((address_space(3)))
#define RLX_AGENT __ATOMIC_RELAXED, __HIP_MEMORY_SCOPE_AGENT
#define INL __attribute__((always_inline))
typedef __amdgpu_buffer_rsrc_t rsrc_t;

constexpr int S_ = 2048, D_ = 512, F_ = 1792;
constexpr int NTHR = 512;
constexpr int AUX_SC1 = 16;
constexpr int RSRC_W3 = 0x00020000;
constexpr float EPS = 1e-5f;
constexpr float LOG2E = 1.44269504088896340736f;

constexpr int WS_FLAG = 0;
constexpr int WS_EPOCH = 16384;
constexpr int WS_O = 65536;
constexpr int WS_H = WS_O + 128 * 16 * 1024;
constexpr int WS_F = WS_H + 128 * 16 * 1024;
constexpr int WS_ST = WS_F + 128 * 56 * 1024;
constexpr int WS_END = WS_ST + 128 * 16 * 128;

constexpr int LDS_BYTES = 163840;
constexpr int L_XOFF = 24576;
constexpr int L_HALO = 131072;
constexpr int L_SCR = 147456;
constexpr int L_SV = L_SCR;
constexpr int L_SK = L_SCR + 10240;
constexpr int L_ST = L_SCR + 14336;
constexpr int L_CS = L_SCR + 14848;
constexpr int L_RED = L_HALO;
constexpr int L_XRES = L_SCR;
constexpr int L_CS3 = LDS_BYTES - 512;
constexpr int w1_addr(int s) { return (s < 4 ? 65536 : 8192) + (s & 3) * 7168; }
constexpr int L_X3 = 65536;
constexpr int w2_quad(int s) { return s >= 9 ? w2_quad(s - 9) : 8192 + 16384 * (s < 2 ? 7 + s : s - 2); }
constexpr int L_RED4 = w2_quad(5);
constexpr int unit_tile(int u, int n) { return u < 2 * n - 6 ? (u & 1) : (u >= 2 * n - 3); }
constexpr int unit_step(int u, int n) { return u < 2 * n - 6 ? (u >> 1) : n - 3 + (u - (2 * n - 6)) % 3; }

constexpr u32 SPIN_CAP = 1u << 17;
constexpr u32 MARK = 0xffffffffu;

template <int I, int N, class F> __device__ __forceinline__ void sfor(F&& f) {
  if constexpr (I < N) { f(std::integral_constant<int, I>{}); sfor<I + 1, N>(f); }
}
template <int V> using IC = std::integral_constant<int, V>;
#define PINV(x) asm volatile("" : "+v"(x))
#define PINS(x) asm volatile("" : "+s"(x))
template <int LO, int SO> __device__ __forceinline__ void dma(u32 lb, u32 vo, rsrc_t rs, u32 sb) {
  u32 t;
  asm volatile("s_add_u32 m0, %1, %5\n\ts_add_u32 %0, %4, %6\n\tbuffer_load_dwordx4 %2, %3, %0 offen lds"
               : "=&s"(t) : "s"(lb), "v"(vo), "s"(rs), "s"(sb), "i"(LO), "i"(SO) : "scc", "memory");
}
template <int O> __device__ __forceinline__ void ldg2(v2u& d, u32 vo, rsrc_t rs, u32 sb) {
  asm volatile("buffer_load_dwordx2 %0, %1, %2, %3 offen offset:%4 sc1" : "=v"(d) : "v"(vo), "s"(rs), "s"(sb), "i"(O) : "memory");
}
template <int O> __device__ __forceinline__ void ldg(v4u& d, u32 vo, rsrc_t rs, u32 sb) {
  asm volatile("buffer_load_dwordx4 %0, %1, %2, %3 offen offset:%4 sc1" : "=v"(d) : "v"(vo), "s"(rs), "s"(sb), "i"(O) : "memory");
}
template <int O> __device__ __forceinline__ void stg(v4u d, u32 vo, rsrc_t rs, u32 sb) {
  asm volatile("buffer_store_dwordx4 %0, %1, %2, %3 offen offset:%4 sc1\n\ts_nop 1" :: "v"(d), "v"(vo), "s"(rs), "s"(sb), "i"(O) : "memory");
}
__device__ __forceinline__ void stg2(v2u d, u32 vo, rsrc_t rs, u32 sb) {
  asm volatile("buffer_store_dwordx2 %0, %1, %2, %3 offen sc1" :: "v"(d), "v"(vo), "s"(rs), "s"(sb) : "memory");
}
__device__ __forceinline__ void touch(u32& d, u32 vo, const void* base) {
  asm volatile("global_load_dword %0, %1, %2" : "+v"(d) : "v"(vo), "s"(base) : "memory");
}
template <int N> __device__ __forceinline__ void waitvm() { asm volatile("s_waitcnt vmcnt(%0)" :: "i"(N) : "memory"); }
#define SB() __builtin_amdgcn_sched_barrier(0)
#define BAR_RAW() do { SB(); asm volatile("" ::: "memory"); __builtin_amdgcn_s_barrier(); asm volatile("" ::: "memory"); SB(); } while (0)
#define BARRIER() do { SB(); asm volatile("s_waitcnt lgkmcnt(0)" ::: "memory"); __builtin_amdgcn_s_barrier(); asm volatile("" ::: "memory"); SB(); } while (0)

__device__ __forceinline__ s16x4 tr4(u32 a) { return __builtin_amdgcn_ds_read_tr16_b64_v4i16((LAS s16x4*)(uintptr_t)a); }
__device__ __forceinline__ s16x8 cat8(s16x4 a, s16x4 b) { return __builtin_shufflevector(a, b, 0, 1, 2, 3, 4, 5, 6, 7); }
template <int T> __device__ __forceinline__ s16x8 trf(u32 a) { return cat8(tr4(a), tr4(a + T)); }
template <class T> __device__ __forceinline__ T lds_ld(u32 a) { return *(LAS T*)(uintptr_t)a; }
template <class T> __device__ __forceinline__ void lds_st(u32 a, T v) { *(LAS T*)(uintptr_t)a = v; }
__device__ __forceinline__ f32x4 mfma32(s16x8 a, v4u b, f32x4 c) {
  return __builtin_amdgcn_mfma_f32_16x16x32_bf16(__builtin_bit_cast(bf8, a), __builtin_bit_cast(bf8, b), c, 0, 0, 0);
}
__device__ __forceinline__ f32x4 mfma16(v2u a, v2u b, f32x4 c) {
  return __builtin_amdgcn_mfma_f32_16x16x16bf16_1k(__builtin_bit_cast(s16x4, a), __builtin_bit_cast(s16x4, b), c, 0, 0, 0);
}
__device__ __forceinline__ u32 pk2(float lo, float hi) {
  bf2 p; p.x = (__bf16)lo; p.y = (__bf16)hi;
  return __builtin_bit_cast(u32, p);
}
__device__ __forceinline__ u32 f2u(float v) { return __builtin_bit_cast(u32, v); }
__device__ __forceinline__ float u2f(u32 v) { return __builtin_bit_cast(float, v); }
__device__ __forceinline__ f32x4 unpk4(u32 a, u32 b) { return f32x4{u2f(a << 16), u2f(a & 0xffff0000u), u2f(b << 16), u2f(b & 0xffff0000u)}; }
__device__ __forceinline__ float max3f(float a, float b, float c) { return __builtin_fmaxf(__builtin_fmaxf(a, b), c); }
template <int N> struct Base {
  u32 v[N];
  __device__ __forceinline__ void set(u32 a) {
#pragma unroll
    for (int i = 0; i < N; ++i) { v[i] = a + i * 57344; PINV(v[i]); }
  }
  template <int OFF> __device__ __forceinline__ u32 at() const { return v[OFF / 57344] + OFF % 57344; }
};
template <bool MAX> __device__ __forceinline__ float red2(float a, float b) {
  auto r = __builtin_amdgcn_permlane16_swap(f2u(a), f2u(b), false, false);
  u32 r0 = r[0], r1 = r[1];
  float v;
  if constexpr (MAX) asm("v_max_f32 %0, %1, %2" : "=v"(v) : "v"(r0), "v"(r1)); else v = u2f(r0) + u2f(r1);
  r = __builtin_amdgcn_permlane32_swap(f2u(v), f2u(v), false, false);
  r0 = r[0]; r1 = r[1];
  if constexpr (MAX) asm("v_max_f32 %0, %1, %2" : "=v"(v) : "v"(r0), "v"(r1)); else v = u2f(r0) + u2f(r1);
  return v;
}
__device__ __forceinline__ void spread2(float v, float& a, float& b) {
  const auto r = __builtin_amdgcn_permlane16_swap(f2u(v), f2u(v), false, false);
  const u32 r0 = r[0], r1 = r[1];
  a = u2f(r0); b = u2f(r1);
}
__device__ __forceinline__ void ln_stats(float s, float q, float& rstd, float& mean) {
  spread2(red2<false>(s, q) * (1.0f / D_), mean, q);
  rstd = __builtin_amdgcn_rsqf(__builtin_fmaxf(q - mean * mean, 0.0f) + EPS);
}
__device__ __forceinline__ v2u ln_pack(f32x4 a, f32x4 cs, float rstd, float mean) {
  const f32x4 t = (a - mean * cs) * rstd;
  return v2u{pk2(t[0], t[1]), pk2(t[2], t[3])};
}
__device__ __forceinline__ u32 unmark(u32 v) { return min(v, MARK - 1u); }
__device__ __forceinline__ void gate(v4u& a0, v4u& a1, u32 vs, u32 vl, rsrc_t rs, u32 ss, u32 s0, u32 s1, u32& budget) {
  u32 smp;
  budget = __builtin_amdgcn_readfirstlane(budget);
  asm volatile(
      "1:\n\t"
      "buffer_load_dword %0, %4, %6, %7 offen sc1\n\t"
      "buffer_load_dwordx4 %1, %5, %6, %8 offen sc1\n\t"
      "buffer_load_dwordx4 %2, %5, %6, %9 offen sc1\n\t"
      "s_waitcnt vmcnt(2)\n\t"
      "v_cmp_eq_u32 vcc, -1, %0\n\t"
      "s_cbranch_vccz 2f\n\t"
      "s_cmp_eq_u32 %3, 0\n\t"
      "s_cbranch_scc1 2f\n\t"
      "s_sub_u32 %3, %3, 1\n\t"
      "s_waitcnt vmcnt(0)\n\t"
      "s_branch 1b\n"
      "2:"
      : "=&v"(smp), "=&v"(a0), "=&v"(a1), "+s"(budget)
      : "v"(vs), "v"(vl), "s"(rs), "s"(ss), "s"(s0), "s"(s1)
      : "vcc", "scc", "memory");
}
__device__ __forceinline__ u64 marked(const v4u& a) {
  const u32 x = a.x, y = a.y, z = a.z, w = a.w;
  return __builtin_amdgcn_ballot_w64(max(max(x, y), max(z, w)) == MARK);
}
template <int N> __device__ __forceinline__ u64 probe(v4u& a) {
  asm volatile("s_waitcnt vmcnt(%1)" : "+v"(a) : "i"(N));
  u64 bad = marked(a);
  PINS(bad);
  return bad;
}
template <int O> __device__ __forceinline__ void mend(u64 bad, v4u& a, u32 vl, rsrc_t rs, u32 sb, u32& budget) {
  if (__builtin_expect(bad != 0ull, 0)) {
    do {
      if (budget == 0) break;
      --budget;
      asm volatile("buffer_load_dwordx4 %0, %1, %2, %3 offen offset:%4 sc1\n\ts_waitcnt vmcnt(0)"
                   : "=&v"(a) : "v"(vl), "s"(rs), "s"(sb), "i"(O) : "memory");
    } while (marked(a) != 0ull);
  }
}
__device__ __forceinline__ void take_stats(v2u (&st)[4], u32 vo, rsrc_t rs, u32 sb, u32& budget) {
  asm volatile("s_waitcnt vmcnt(5)" : "+v"(st[0]), "+v"(st[1]), "+v"(st[2]), "+v"(st[3]) :: "memory");
  while (__builtin_expect((marked(v4u{st[0].x, st[0].y, st[1].x, st[1].y}) | marked(v4u{st[2].x, st[2].y, st[3].x, st[3].y})) != 0ull, 0)) {
    if (budget == 0) break;
    --budget;
    asm volatile("buffer_load_dwordx2 %0, %4, %5, %6 offen sc1\n\tbuffer_load_dwordx2 %1, %4, %5, %6 offen offset:128 sc1\n\t"
                 "buffer_load_dwordx2 %2, %4, %5, %6 offen offset:256 sc1\n\tbuffer_load_dwordx2 %3, %4, %5, %6 offen offset:384 sc1\n\t"
                 "s_waitcnt vmcnt(0)" : "=&v"(st[0]), "=&v"(st[1]), "=&v"(st[2]), "=&v"(st[3]) : "v"(vo), "s"(rs), "s"(sb) : "memory");
  }
}
__device__ __forceinline__ void flags_ok(v2u& chk, v2u tag, bool use, u32 vo, rsrc_t rs, u32& budget) {
  for (;;) {
    const u32 c0 = chk.x, c1 = chk.y, t0 = tag.x, t1 = tag.y;
    if (__builtin_expect(__builtin_amdgcn_ballot_w64(use && (c0 != t0 || c1 != t1)) == 0ull || budget == 0, 1)) break;
    --budget;
    asm volatile("buffer_load_dwordx2 %0, %1, %2, 0 offen sc1\n\ts_waitcnt vmcnt(0)" : "=&v"(chk) : "v"(vo), "s"(rs) : "memory");
  }
}

#define NOMEM 64
#define TOUCH_W(n) ((NOMEM & 64) ? 99 : (n))
#define DBG_SZ(bit, n) ((NOMEM & (bit)) ? 0 : (n))
#define STAMP(k) do { } while (0)

__global__ void __launch_bounds__(NTHR, 2)
fused_block(const u16* x_, const u16* wqkv_, const u16* wo_, const u16* w1_, const u16* w2_, u16* out_, unsigned char* ws_) {
  STAMP(0);
  __builtin_amdgcn_s_setprio(3);
  v8u ka0; v4u ka1; v2u ka2;
  asm volatile("s_load_dwordx8 %0, %3, 0x0\n\ts_load_dwordx4 %1, %3, 0x20\n\ts_load_dwordx2 %2, %3, 0x30"
               : "=&s"(ka0), "=&s"(ka1), "=&s"(ka2) : "s"(__builtin_amdgcn_kernarg_segment_ptr()) : "memory");
  const int tid = threadIdx.x, lane = tid & 63, li = lane & 15, g = lane >> 4;
  const int w = __builtin_amdgcn_readfirstlane(tid >> 6);
  const int b = blockIdx.x, xg = b & 7, sl = b >> 3;
  const int pp1 = 8 * (xg >> 1) + (sl >> 2), hq = 4 * (xg & 1) + (sl & 3);
  const int pp = 4 * xg + (sl >> 3), cb = sl & 7;
  const int p3 = 4 * (xg >> 1) + (sl >> 3), j = 8 * (xg & 1) + (sl & 7);
  const int rt = w & 3, kh = w >> 2;
  const int RT2 = 4 * pp + rt;
  const int T1 = 8 * p3 + 2 * rt + kh;

  u32 pf_dst = 0;
  if (w == 99) {
    u64 pc; u32 len;
    asm volatile("s_getpc_b64 %0\n.Lfb_pc:\n\ts_mov_b32 %1, .Lfb_end-.Lfb_pc" : "=s"(pc), "=s"(len));
    const u32 off = min((u32)(((sl & 3) * 64 + lane) * 128), min(len, 65536u) & ~3u);
    touch(pf_dst, off, (const void*)pc);
  }
  const int r8 = lane >> 3, u8 = lane & 7;
  const u32 colsw = (u32)((16 * ((u8 >> 1) ^ ((r8 >> 1) & 3)) + 8 * (u8 & 1)) * 2);
  u32 voB = (u32)((8 * (r8 >> 2) + (r8 & 3)) * 3072) + colsw;
  u32 voX = (u32)(r8 * 1024 + ((u8 ^ g) * 16));
  u32 voXw = voX ^ (u32)((w & 1) * 64);
  u32 lw = (u32)(w * 1024);
  const int xq = xg >> 1;
  u32 sbX[4], sbQ[4];
#pragma unroll
  for (int i = 0; i < 4; ++i) {
    sbX[i] = (u32)((64 * pp1 + 8 * w) * 1024 + ((i + xq) & 3) * 256);
    sbQ[i] = (u32)((128 * ((i + xq) & 3) + 32 * (w >> 2) + 16 * (w & 1) + 4 * ((w >> 1) & 1)) * 3072 + hq * 128);
  }
  u32 hr = (u32)(max(64 * pp1 - 16, 0) * 1024 + ((w + 2 * xq) & 7) * 128);
  u32 vtag = (u32)(8 * ((lane >= 8 && lane < 24) ? p3 : lane == 40 ? (pp1 >> 1) : (pp >> 1)));
  u32 vt0 = w < 3 ? (u32)(((16 * w + (lane >> 2)) * 32 + sl) * 1024 + (xg & 1) * 512 + (lane & 3) * 128)
                  : (u32)(xq * 524288 + ((8 * (w - 3) + r8) * 32 + sl) * 1024 + u8 * 128);
  PINV(vt0); PINV(voB); PINV(voX); PINV(voXw); PINV(vtag); PINS(lw); PINS(sbX[0]); PINS(sbQ[0]); PINS(hr);
  SB();
  asm volatile("s_waitcnt lgkmcnt(0)" : "+s"(ka0), "+s"(ka1), "+s"(ka2) :: "memory");
  auto ptr = [](u32 lo, u32 hi) INL { return (void*)(((u64)hi << 32) | lo); };
  void* const x = ptr(ka0[0], ka0[1]); void* const wqkv = ptr(ka0[2], ka0[3]); void* const wo = ptr(ka0[4], ka0[5]);
  void* const w1 = ptr(ka0[6], ka0[7]); void* const w2 = ptr(ka1[0], ka1[1]); void* const out = ptr(ka1[2], ka1[3]);
  unsigned char* const ws = (unsigned char*)ptr(ka2[0], ka2[1]);
  rsrc_t rsX = __builtin_amdgcn_make_buffer_rsrc(x, 0, DBG_SZ(1, S_ * D_ * 2), RSRC_W3);
  rsrc_t rsQ = __builtin_amdgcn_make_buffer_rsrc(wqkv, 0, DBG_SZ(2, D_ * 3 * D_ * 2), RSRC_W3);
  auto chunk = [&](auto C_, auto I_) INL {
    constexpr int c = C_.value, i = I_.value;
    if constexpr (i == 0) dma<(c & 3) * 32768 + L_XOFF, (c & 1) * 128>(lw, voXw, rsX, sbX[c >> 1]);
    else dma<(c & 3) * 32768 + (i - 1) * 8192, (c & 1) * 64 * 3072 + (i - 1) * 1024>(lw, voB, rsQ, sbQ[c >> 1]);
  };
  sfor<0, 4>([&](auto I_) INL { chunk(IC<0>{}, I_); });
  dma<L_HALO, 0>(2 * lw, voX, rsX, hr);
  dma<L_HALO + 1024, 8192>(2 * lw, voX ^ 64u, rsX, hr);
  rsrc_t rsW = __builtin_amdgcn_make_buffer_rsrc((void*)ws, 0, WS_END, RSRC_W3);
  v2u tag;
  ldg2<0>(tag, vtag, rsW, (u32)WS_EPOCH);
  __builtin_amdgcn_s_setprio(2);
  if (w < ((NOMEM & 128) ? 0 : 5)) touch(pf_dst, vt0, w < 3 ? wqkv : x);
  sfor<0, 4>([&](auto I_) INL { chunk(IC<1>{}, I_); });
  __builtin_amdgcn_s_setprio(1);
  SB();

  rsrc_t rsO = __builtin_amdgcn_make_buffer_rsrc(wo, 0, DBG_SZ(4, D_ * D_ * 2), RSRC_W3);
  rsrc_t rs1 = __builtin_amdgcn_make_buffer_rsrc(w1, 0, DBG_SZ(8, D_ * F_ * 2), RSRC_W3);
  rsrc_t rs2 = __builtin_amdgcn_make_buffer_rsrc(w2, 0, DBG_SZ(16, F_ * D_ * 2), RSRC_W3);
  rsrc_t rsOut = __builtin_amdgcn_make_buffer_rsrc(out, 0, S_ * D_ * 2, RSRC_W3);
  PINS(rsO); PINS(rs1); PINS(rs2); PINS(rsOut); PINS(rsW);
#define rsM rsW
  u32 budget = SPIN_CAP;
  const int odd = j & 1, swp = (sl >> 3) & 1;
  u32 vlane = (u32)(lane * 16);
  u32 vo64 = (u32)(r8 * 1024) + colsw;
  u32 vo1;
  { const int chi = 64 * (w < 7 ? w : 0) + lane; vo1 = (u32)((chi / 14) * 3584 + (chi % 14) * 16); }
  u32 lw1 = (u32)((w < 7 ? w : 0) * 1024);
  u32 sb1[2] = {(u32)(4 * swp * 114688 + 224 * j), (u32)(4 * (swp ^ 1) * 114688 + 224 * j)};
  auto dma_w1 = [&](auto B_, auto I_) INL {
    constexpr int m = B_.value, i = I_.value;
    dma<w1_addr(4 * m) + i * 7168, (8 * (i >> 2) + (i & 3)) * 114688>(lw1, vo1, rs1, sb1[m]);
  };
  u32 sbO = (u32)(w * 8192 + cb * 128);
  u32 so_o = (u32)(WS_O + ((4 * pp1 + 2 * kh) * 16 + 2 * hq + (rt >> 1)) * 1024 + (rt & 1) * 8);
  u32 so_h = (u32)(WS_H + (RT2 * 16 + 2 * cb + kh) * 1024), so_st = (u32)(WS_ST + (RT2 * 16 + 2 * cb + kh) * 128);
  u32 so_f = (u32)(WS_F + (T1 * 56 + 7 * (j >> 1) + 4 * odd) * 1024), so_f3 = (u32)(WS_F + (T1 * 56 + 7 * (j >> 1) + 3) * 1024 + 8 * odd);
  u32 vchk = (u32)(8 * (lane < 8 ? pp * 16 + lane : lane < 24 ? 512 + p3 * 16 + (lane - 8) : 768 + (pp >> 1) * 16 + ((lane - 24) & 15)));
  u32 vflag = (u32)(8 * (lane == 0 ? 512 + pp * 8 + cb : lane == 8 ? 768 + p3 * 16 + j : pp1 * 16 + hq));
  PINV(vlane); PINV(vo64); PINV(vo1); PINV(vchk); PINV(vflag);
  PINS(lw1); PINS(sb1[0]); PINS(sb1[1]); PINS(sbO); PINS(so_o); PINS(so_h); PINS(so_st); PINS(so_f); PINS(so_f3);
  u32 vt1 = rt == 1 ? (u32)(sl * 16384 + kh * 8192 + lane * 128) : w == 6 ? (u32)(b * 7168 + min(lane, 55) * 128)
          : w == 7 ? (u32)(64 * pp * 1024) : (u32)((16 * sl + 4 * (w == 0 ? 3 : w - 2) + g) * 3584 + (xg & 1) * 1792 + min(li, 13) * 128);
  const void* pt1 = rt == 1 ? wo : w == 6 ? w2 : w == 7 ? out : w1;
  int site = TOUCH_W(w == 0 ? 5 : w == 4 ? 4 : rt);
  PINV(vt1); PINS(pt1); PINS(site);
#pragma unroll
  for (int i = 1; i < 4; ++i) { PINS(sbX[i]); PINS(sbQ[i]); }
  v4u mark4 = {MARK, MARK, MARK, MARK};
  v4u ones = {0x3f803f80u, 0x3f803f80u, 0x3f803f80u, 0x3f803f80u};
  PINV(mark4); PINV(ones);

  SB();
  sfor<0, 4>([&](auto I_) INL { chunk(IC<2>{}, I_); });
  __builtin_amdgcn_s_setprio(0);
  SB();
  const int q4 = (lane >> 2) & 3, p4 = lane & 3;
  const u32 sw64 = (2 * g + (q4 >> 1)) & 3;
  u32 trb = (4 * g + q4) * 128 + 8 * p4;
  Base<3> bqa; bqa.set(trb + 32 * (rt ^ sw64));
  u32 ab[2] = {(u32)(li * 128 + ((g ^ (li >> 1)) * 16)), (u32)(li * 128 + ((g ^ (li >> 1)) * 16)) ^ 64u};
  Base<2> aa[2]; aa[0].set(ab[0] + L_XOFF + kh * 4096); aa[1].set(ab[1] + L_XOFF + kh * 4096);
  u32 bw[4];
#pragma unroll
  for (int c = 0; c < 4; ++c) { bw[c] = trb + 32 * (c ^ (2 * kh) ^ sw64); PINV(bw[c]); }
  f32x4 bias_own, bias_prev, bias_prev0;
  v4u pmask;
  {
    const int par = li & 1;
    const bool ownA = (4 * g + par) <= li, ownB = (4 * g + par + 2) <= li, first = (pp1 == 0) && (kh == 0);
#pragma unroll
    for (int r = 0; r < 4; ++r) {
      const bool own = (r & 2) ? ownB : ownA, ok = (r & 1) == par;
      bias_own[r] = (ok && own) ? 0.f : -1e30f;
      bias_prev[r] = (ok && !own) ? 0.f : -1e30f;
      bias_prev0[r] = (ok && !own && !first) ? 0.f : -1e30f;
    }
    const u32 hm = par ? 0xffff0000u : 0x0000ffffu;
    pmask = v4u{ownA ? 0u : hm, ownB ? 0u : hm, ownA ? hm : 0u, ownB ? hm : 0u};
  }
  PINV(bias_own); PINV(bias_prev); PINV(bias_prev0); PINV(pmask);
  u32 a_st = (u32)(L_ST + li * 8), a_cs = (u32)(L_CS + rt * 192 + g * 16), a_vw = (u32)(L_SV + rt * 2560 + li * 32 + g * 8);
  u32 a_l8 = (u32)(L_SV + lane * 8);
  PINV(a_st); PINV(a_cs); PINV(a_vw); PINV(a_l8);

  f32x4 acc[14];
#pragma unroll
  for (int c = 0; c < 10; ++c) acc[c] = f32x4{0.f, 0.f, 0.f, 0.f};
  v4u res;
  v2u chk;
  {
    s16x8 fb[2][3];
    v4u fa[2][3];
    float rsh, mnh;
    auto rd = [&](auto H_, auto N_, u32 t3) INL {
      constexpr int H = H_.value, N = N_.value, SL = ((H >> 1) & 3) * 32768, e = H & 1;
      if constexpr (N < 3) fb[e][N] = trf<2048>(bqa.at<SL + N * 8192 + e * 4096>());
      else if constexpr (N < 5) fa[e][N - 3] = lds_ld<v4u>(aa[e].template at<SL + (N - 3) * 2048>());
      else fa[e][2] = lds_ld<v4u>(t3);
    };
    auto third = [&](int H) INL {
      return ab[H & 1] + (kh ? (u32)(((H >> 1) & 3) * 32768 + L_XOFF + rt * 2048) : (u32)(L_HALO + (H >> 1) * 2048));
    };
    auto aux = [&](int e) INL {
      acc[9] = mfma32(__builtin_bit_cast(s16x8, fa[e][2]), fa[e][2], acc[9]);
      if (kh == 0) {
#pragma unroll
        for (int m = 0; m < 3; ++m) acc[6 + m] = mfma32(fb[e][m], fa[e][2], acc[6 + m]);
      } else {
        acc[6] = mfma32(__builtin_bit_cast(s16x8, ones), fa[e][2], acc[6]);
      }
    };
    auto stream = [&](auto C_, auto I_) INL {
      constexpr int C = C_.value, i = I_.value;
      if constexpr (i == 5 && C >= 1 && C <= 5) { if (site == C) touch(pf_dst, vt1, pt1); }
      if constexpr (C <= 4) {
        if constexpr (i >= 1 && i <= 4) chunk(IC<C + 3>{}, IC<i - 1>{});
        if constexpr (i == 0 && C == 1) stg<0>(mark4, vlane, rsM, (u32)(WS_O + ((4 * pp1 + (w >> 1)) * 16 + 2 * hq + (w & 1)) * 1024));
        if constexpr (i == 0 && C == 2) stg<0>(mark4, vlane, rsM, so_h);
        if constexpr (i == 0 && C == 3) { if (lane < 8) stg<0>(mark4, vlane, rsM, so_st); }
        if constexpr (i == 0 && C == 4) dma<L_XRES, 0>(lw, voXw, rsX, (u32)((64 * pp + 8 * w) * 1024 + cb * 128));
      } else {
        if constexpr (i < 4 && C <= 6) dma<(C - 5) * 32768 + i * 8192, ((C - 5) * 32 + 8 * i) * 8192>(lw, vo64, rsO, sbO);
        if constexpr (i < 4 && C == 7) dma_w1(IC<0>{}, I_);
        if constexpr (i == 4) stg<(C - 5) * 1024>(mark4, vlane, rsM, so_f);
        if constexpr (i == 5 && C == 7) stg2(v2u{MARK, MARK}, vlane, rsM, so_f3);
      }
    };
    SB();
    if (w < ((NOMEM & 128) ? 0 : 5)) waitvm<9>(); else waitvm<8>();
    PINV(tag);
    if (lane < 8) lds_st<v4u>(L_HALO + 2 * lw + vlane, ones);
    { const u64 t = (((u64)tag.y << 32) | tag.x) + 1ull; tag = v2u{(u32)t, (u32)(t >> 32)}; }
    BARRIER();
    STAMP(1);
    { const u32 t3 = third(0); sfor<0, 6>([&](auto N_) INL { rd(IC<0>{}, N_, t3); }); }
    SB();
    sfor<0, 16>([&](auto H_) INL {
      constexpr int H = H_.value, e = H & 1, C = (H + 1) >> 1;
      u32 t3 = 0;
      if constexpr (H < 15) t3 = third(H + 1);
      if constexpr (e && C < 8) {
        waitvm<(C == 1 ? 8 : C == 2 ? 9 : 10)>();
        BARRIER();
        if constexpr (C == 6) { if (w == 0 && (lane == 0 || lane == 40)) stg2(tag, vflag, rsW, 0u); }
      }
      if constexpr (H == 15) { aux(e); SB(); }
      sfor<0, 6>([&](auto N_) INL {
        constexpr int N = N_.value;
        acc[N] = mfma32(fb[e][N % 3], fa[e][N / 3], acc[N]);
        if constexpr (H < 15) rd(IC<H + 1>{}, N_, t3);
        SB();
        if constexpr (H == 0 && N >= 1 && N <= 4) { stream(IC<0>{}, N_); SB(); }
        if constexpr (H >= 1 && H < 15 && (N & 1)) { stream(IC<C>{}, IC<(e ? 0 : 3) + N / 2>{}); SB(); }
        if constexpr (H == 15 && N == 1) {
          const float dg = (li & 2) ? ((li & 1) ? acc[9][3] : acc[9][2]) : ((li & 1) ? acc[9][1] : acc[9][0]);
          ln_stats(g == 0 ? (kh ? acc[6][0] : acc[9][0]) : 0.f, g == (li >> 2) ? dg : 0.f, rsh, mnh);
          SB();
        }
        if constexpr (H == 15 && N == 3) {
          if (kh) {
            if (g == 0) lds_st<f32x2>(a_st + rt * 128, f32x2{rsh, mnh});
          } else if (li == 0) {
#pragma unroll
            for (int m = 0; m < 3; ++m) lds_st<f32x4>(a_cs + m * 64, acc[6 + m]);
          }
          SB();
        }
      });
      if constexpr (H < 15) { aux(e); SB(); }
      if constexpr (H == 13) {
        const u32 ra = L_XRES + rt * 2048 + li * 128 + (g & 1) * 8;
        const v2u r0 = lds_ld<v2u>(ra + (((4 * kh + (g >> 1)) ^ (li >> 1)) * 16));
        const v2u r1 = lds_ld<v2u>(ra + (((4 * kh + 2 + (g >> 1)) ^ (li >> 1)) * 16));
        res = v4u{r0.x, r0.y, r1.x, r1.y};
        SB();
      }
    });
    STAMP(2);
    BARRIER();
    STAMP(3);
    const f32x2 sta = lds_ld<f32x2>(a_st + kh * 256), stb = lds_ld<f32x2>(a_st + kh * 256 + 128);
    const f32x4 csv = lds_ld<f32x4>(a_cs + 128), csk = lds_ld<f32x4>(a_cs + 64), csq = lds_ld<f32x4>(a_cs);
    SB();
    ldg2<0>(chk, vchk, rsW, 0u);
    SB();
    lds_st<v2u>(a_vw + kh * 1024 + 512, ln_pack(acc[2], csv, sta.x, sta.y));
    lds_st<v2u>(a_vw + kh * 1024 + 1024, ln_pack(acc[5], csv, stb.x, stb.y));
    const v2u kb = ln_pack(acc[4], csk, stb.x, stb.y);
    if (kh == 0) {
      lds_st<v2u>(a_vw, ln_pack(acc[8], csv, rsh, mnh));
      lds_st<v2u>(a_l8 + (L_SK - L_SV) + lw, ln_pack(acc[7], csk, rsh, mnh));
      lds_st<v2u>(a_l8 + (L_SK - L_SV) + lw + 512, kb);
    }
    SB();
    const v2u ka = ln_pack(acc[1], csk, sta.x, sta.y);
    const f32x4 qfa = acc[0] - sta.y * csq, qfb = acc[3] - stb.y * csq;
    const v2u qa = {pk2(qfa[0], qfa[1]), pk2(qfa[2], qfa[3])}, qb = {pk2(qfb[0], qfb[1]), pk2(qfb[2], qfb[3])};
    const f32x4 s1a = mfma16(ka, qa, bias_own), s0b = mfma16(ka, qb, bias_prev), s1b = mfma16(kb, qb, bias_own);
    const float ca = sta.x * (0.25f * LOG2E), cbq = stb.x * (0.25f * LOG2E);
    waitvm<8>();
    PINV(pf_dst);
    BARRIER();
    const v2u kp = lds_ld<v2u>(a_l8 + (L_SK - L_SV) + rt * 1024 + kh * 512);
    const u32 vr = a_l8 + rt * 2560 + kh * 1024;
    const s16x8 vta = trf<512>(vr);
    s16x4 vtb0 = tr4(vr + 512); PINV(vtb0);
    const s16x8 vtb = cat8(vtb0, tr4(vr + 1024));
    const float bA = __builtin_fmaxf(max3f(s0b[0], s0b[1], s1b[0]), s1b[1]), bB = __builtin_fmaxf(max3f(s0b[2], s0b[3], s1b[2]), s1b[3]);
    const f32x4 s0a = mfma16(kp, qa, bias_prev0);
    const float aA = __builtin_fmaxf(max3f(s0a[0], s0a[1], s1a[0]), s1a[1]), aB = __builtin_fmaxf(max3f(s0a[2], s0a[3], s1a[2]), s1a[3]);
    float ma, mb;
    spread2(red2<true>(__builtin_fmaxf(aA, aB), __builtin_fmaxf(bA, bB)), ma, mb);
    const float eaA = __builtin_amdgcn_exp2f((aA - ma) * ca), eaB = __builtin_amdgcn_exp2f((aB - ma) * ca);
    const float ebA = __builtin_amdgcn_exp2f((bA - mb) * cbq), ebB = __builtin_amdgcn_exp2f((bB - mb) * cbq);
    float ia, ib;
    spread2(__builtin_amdgcn_rcpf(red2<false>(eaA + eaB, ebA + ebB)), ia, ib);
    const u32 paA = pk2(eaA * ia, eaA * ia), paB = pk2(eaB * ia, eaB * ia), pbA = pk2(ebA * ib, ebA * ib), pbB = pk2(ebB * ib, ebB * ib);
    const f32x4 z = {0.f, 0.f, 0.f, 0.f};
    const f32x4 oa_ = mfma32(vta, v4u{paA, paB, paA, paB} & pmask, z), ob_ = mfma32(vtb, v4u{pbA, pbB, pbA, pbB} & pmask, z);
    stg2(v2u{unmark(pk2(oa_[0], oa_[1])), unmark(pk2(oa_[2], oa_[3]))}, vlane, rsW, so_o);
    stg2(v2u{unmark(pk2(ob_[0], ob_[1])), unmark(pk2(ob_[2], ob_[3]))}, vlane, rsW, so_o + 16384);
  }
  SB();
  STAMP(4);

  {
    s16x8 bo[8][4];
    u32 bk[4];
#pragma unroll
    for (int c = 0; c < 4; ++c) bk[c] = bw[c] + kh * 32768;
    sfor<0, 8>([&](auto S_c) INL {
      constexpr int s = S_c.value;
      if constexpr (s < 4) dma_w1(IC<0>{}, IC<s + 4>{});
#pragma unroll
      for (int c = 0; c < 4; ++c) bo[s][c] = trf<2048>(bk[c] + s * 4096);
      SB();
    });
    acc[0] = unpk4(res.x, res.y); acc[1] = unpk4(res.z, res.w);
    acc[2] = acc[3] = f32x4{0.f, 0.f, 0.f, 0.f};
    const u32 ob[2] = {(u32)(WS_O + (RT2 * 16 + 8 * kh) * 1024), (u32)(WS_O + (RT2 * 16 + 8 * kh + 4) * 1024)};
    const u32 xw = L_RED + 2 * lw + vlane, xr = L_RED + 2 * (lw ^ 4096u) + vlane;
    asm volatile("s_waitcnt vmcnt(6)" : "+v"(chk) :: "memory");
    flags_ok(chk, tag, lane < 24, vchk, rsW, budget);
    SB();
    STAMP(5);
    v4u oa[8];
    gate(oa[0], oa[1], (u32)(((lane >> 1) & 7) * 1024 + 1012 + (lane & 1) * 8), vlane, rsW, ob[0], ob[0], ob[0] + 1024, budget);
    STAMP(6);
    sfor<2, 8>([&](auto S_c) INL { ldg<(S_c.value & 3) * 1024>(oa[S_c.value], vlane, rsW, ob[S_c.value >> 2]); });
    SB();
    sfor<0, 8>([&](auto S_c) INL {
      constexpr int s = S_c.value;
      mend<(s & 3) * 1024>(probe<7 - s>(oa[s]), oa[s], vlane, rsW, ob[s >> 2], budget);
#pragma unroll
      for (int c = 0; c < 4; ++c) acc[c ^ 2] = mfma32(bo[s][c ^ 2], oa[s], acc[c ^ 2]);
      SB();
    });
    STAMP(7);
    lds_st<f32x4>(xw, acc[2]);
    lds_st<f32x4>(xw + 1024, acc[3]);
    BARRIER();
    const f32x4 k0 = acc[0] + lds_ld<f32x4>(xr), k1 = acc[1] + lds_ld<f32x4>(xr + 1024);
    res = v4u{unmark(pk2(k0[0], k0[1])), unmark(pk2(k0[2], k0[3])), unmark(pk2(k1[0], k1[1])), unmark(pk2(k1[2], k1[3]))};
    stg<0>(res, vlane, rsW, so_h);
    SB();
    const f32x4 h0 = unpk4(res.x, res.y), h1 = unpk4(res.z, res.w);
    const f32x4 hs = h0 + h1, hq2 = h0 * h0 + h1 * h1;
    const float sq = red2<false>((hs[0] + hs[1]) + (hs[2] + hs[3]), (hq2[0] + hq2[1]) + (hq2[2] + hq2[3]));
    if (lane < 32)
      asm volatile("buffer_store_dword %0, %1, %2, %3 offen sc1" :: "v"(unmark(f2u(sq))), "v"((u32)(li * 8 + g * 4)), "s"(rsW), "s"(so_st) : "memory");
    if (w == 0 && lane == 8) stg2(tag, vflag, rsW, 0u);
  }
  SB();
  STAMP(8);

  s16x8 b4[8][4];
  Base<3> bb[4];
  auto rd4 = [&](auto S_c, int c) INL { b4[S_c.value & 7][c] = trf<2048>(bb[c].template at<w2_quad(S_c.value)>()); };
  const u32 sb2[2] = {(u32)(((14 * (w >> 1) + 7 * swp) * 32 + 16 * (w & 1)) * 1024 + cb * 128),
                      (u32)(((14 * (w >> 1) + 7 * (swp ^ 1)) * 32 + 16 * (w & 1)) * 1024 + cb * 128)};
  auto dma_w2 = [&](auto N_) INL {
    constexpr int n = N_.value, s = n >> 1;
    dma<w2_quad(s) + (n & 1) * 1024, (s % 7) * 32768 + (n & 1) * 8192>(2 * lw, vo64, rs2, sb2[s / 7]);
  };
  {
    const u32 wb = (4 * g + q4) * 224 + 8 * p4;
    Base<2> wbm, wbl;
    wbm.set(wb + 32 * odd + kh * 28672); wbl.set(wb + (odd ? 0 : 192) + kh * 28672);
    s16x4 bq[4][7][2];
    auto rd3 = [&](auto S_c, auto N_) INL {
      constexpr int s = S_c.value, c = N_.value >> 1, t = N_.value & 1;
      if constexpr (c < 6) bq[s & 3][c][t] = tr4(wbm.at<w1_addr(s) + 32 * c + 3584 * t>());
      else bq[s & 3][c][t] = tr4(wbl.at<w1_addr(s) + 3584 * t>());
    };
    Base<3> ca; ca.set(wb + ((w < 6) ? 32 * (odd + w) : (odd ? 0 : 192)));
    f32x4 csum = {0.f, 0.f, 0.f, 0.f};
    s16x8 ct[2];
    sfor<0, 8>([&](auto I_) INL {
      constexpr int i = I_.value;
      dma_w1(IC<1>{}, I_);
      if constexpr (i >= 2) csum = mfma32(ct[i & 1], ones, csum);
      ct[i & 1] = trf<3584>(ca.at<65536 + i * 7168>());
      sfor<0, 7>([&](auto N_) INL { rd3(IC<(i >> 1)>{}, IC<7 * (i & 1) + N_.value>{}); });
      SB();
    });
    csum = mfma32(ct[0], ones, csum);
    csum = mfma32(ct[1], ones, csum);
#pragma unroll
    for (int c = 0; c < 14; ++c) acc[c] = f32x4{0.f, 0.f, 0.f, 0.f};
    v4u ha[16];
    v2u st[4];
    u32 hb[2][2];
#pragma unroll
    for (int t = 0; t < 2; ++t)
#pragma unroll
      for (int m = 0; m < 2; ++m) { hb[t][m] = (u32)(WS_H + ((T1 ^ t ^ 1) * 16 + 8 * kh + 4 * (m ^ swp)) * 1024); PINS(hb[t][m]); }
    const u32 vst = (u32)(g * 512 + li * 8), sst = (u32)(WS_ST + T1 * 2048);
    const u32 xw = L_X3 + w * 7168 + vlane, xr = L_X3 + (w ^ 4) * 7168 + vlane, cr = L_CS3 + g * 16;
    SB();
    STAMP(9);
    gate(ha[0], ha[1], (u32)(((lane >> 3) & 1) * 16384 + u8 * 1024 + 1020), vlane, rsW, (u32)(WS_H + ((T1 & ~1) * 16 + 8 * kh) * 1024),
         hb[0][0], hb[1][0], budget);
    STAMP(10);
#define HLD(u) (unit_step(u, 8) & 3) * 1024
#define HSB(u) hb[unit_tile(u, 8)][unit_step(u, 8) >> 2]
    sfor<2, 8>([&](auto U_) INL { constexpr int u = U_.value; ldg<HLD(u)>(ha[u], vlane, rsW, HSB(u)); });
    SB();
    constexpr auto nops = [](int t) { return t == 7 ? 5 : t < 8 ? 1 : t == 8 ? 3 : t == 9 ? 2 : 1; };
    constexpr auto younger = [nops](int uu) {
      int n = uu < 8 ? 7 - uu : nops(uu - 8) - 1;
      for (int t = (uu < 8 ? 0 : uu - 7); t < uu; ++t) n += nops(t);
      return n;
    };
    u64 bad = probe<7>(ha[0]);
    float rstd, mean;
    f32x4 cs[2];
    sfor<0, 16>([&](auto U_) INL {
      constexpr int u = U_.value, tl = unit_tile(u, 8), s = unit_step(u, 8);
      mend<HLD(u)>(bad, ha[u], vlane, rsW, HSB(u), budget);
      if constexpr (u == 2) BAR_RAW();
      if constexpr (u == 10) BARRIER();
      sfor<0, 7>([&](auto C_) INL {
        constexpr int c = C_.value;
        acc[7 * tl + c] = mfma32(cat8(bq[s & 3][c][0], bq[s & 3][c][1]), ha[u], acc[7 * tl + c]);
        if constexpr (u >= 2 && u < 10) rd3(IC<(u >> 1) + 3>{}, IC<7 * (u & 1) + c>{});
        if constexpr (u >= 2 && u < 7 && (c == 2 || c == 5)) {
          if constexpr (u >= 3) csum = mfma32(ct[c & 1], ones, csum);
          if constexpr (u < 6) ct[c & 1] = trf<3584>(ca.at<8192 + (2 * (u - 2) + (c & 1)) * 7168>());
        }
        if constexpr (u >= 13 && (7 * (u - 13) + c) % 3 == 1) lds_st<f32x4>(xw + ((7 * (u - 13) + c) / 3) * 1024, acc[(7 * (u - 13) + c) / 3]);
        if constexpr (u == 11 || u == 12) {
          constexpr int i = 7 * (u - 11) + c;
          if constexpr ((i & 1) == 0) cs[(i >> 1) & 1] = lds_ld<f32x4>(cr + (i >> 1) * 64);
          if constexpr (i >= 3 && (i & 1)) { acc[7 + (i - 3) / 2] -= mean * cs[((i - 3) / 2) & 1]; PINV(acc[7 + (i - 3) / 2]); }
        }
        SB();
        if constexpr (c == 0 && u < 8) ldg<HLD(u + 8)>(ha[u + 8], vlane, rsW, HSB(u + 8));
        if constexpr (u == 7 && c >= 1 && c <= 4) ldg2<(c - 1) * 128>(st[c - 1], vst, rsW, sst);
        if constexpr (u == 8 && c == 0) ldg2<0>(chk, vchk, rsW, 0u);
        if constexpr ((u == 8 || u == 9) && (c == 2 || c == 4)) dma_w2(IC<2 * (u - 8) + (c >> 2)>{});
        if constexpr (u >= 10 && c == 2) dma_w2(IC<u - 6>{});
        if constexpr (u == 8 && c == 3) { if (li == 0 && w < 7) lds_st<f32x4>(L_CS3 + w * 64 + g * 16, csum); }
        if constexpr (u == 10 && c == 0) {
          take_stats(st, vst, rsW, sst, budget);
        }
        if constexpr (u == 10 && c == 3) {
          const f32x2 t = (__builtin_bit_cast(f32x2, st[0]) + __builtin_bit_cast(f32x2, st[1])) +
                          (__builtin_bit_cast(f32x2, st[2]) + __builtin_bit_cast(f32x2, st[3]));
          ln_stats(t.x, t.y, rstd, mean);
          PINV(rstd); PINV(mean);
        }
        if constexpr (c == 5 && u < 15) bad = probe<younger(u + 1)>(ha[u + 1]);
        SB();
      });
      if constexpr (u == 12) { acc[13] -= mean * cs[0]; PINV(acc[13]); SB(); }
    });
    STAMP(11);
    BARRIER();
    f32x4 part[7];
#pragma unroll
    for (int c = 0; c < 7; ++c) part[c] = lds_ld<f32x4>(xr + c * 1024);
#pragma unroll
    for (int c = 0; c < 4; ++c) bb[c].set((bw[0] ^ (u32)(32 * c)) + (2 * (rt & 1) + kh) * 4096);
    SB();
    const float c1 = -LOG2E * rstd;
    u32 fw[14];
    sfor<0, 7>([&](auto C_) INL {
      constexpr int c = C_.value;
      const f32x4 v = (acc[7 + c] + part[c]) * c1;
      float y[4];
#pragma unroll
      for (int r = 0; r < 4; ++r) y[r] = v[r] * __builtin_amdgcn_rcpf(__builtin_fmaf(__builtin_amdgcn_exp2f(v[r]), -LOG2E, -LOG2E));
      fw[2 * c] = unmark(pk2(y[0], y[1]));
      fw[2 * c + 1] = unmark(pk2(y[2], y[3]));
      if constexpr (c & 1) stg<(c >> 1) * 1024>(v4u{fw[2 * c - 2], fw[2 * c - 1], fw[2 * c], fw[2 * c + 1]}, vlane, rsW, so_f);
      if constexpr (c == 6) stg2(v2u{fw[12], fw[13]}, vlane, rsW, so_f3);
      SB();
      if constexpr (c == 1) {
        waitvm<7>();
        BARRIER();
      }
      if constexpr (c >= 1 && c <= 4) {
        dma_w2(IC<8 + 2 * c>{}); dma_w2(IC<9 + 2 * c>{});
        rd4(IC<((c - 1) >> 1)>{}, 2 * ((c - 1) & 1)); rd4(IC<((c - 1) >> 1)>{}, 2 * ((c - 1) & 1) + 1);
        SB();
      }
    });
  }
  SB();
  STAMP(12);

  {
    waitvm<12>();
    BAR_RAW();
    sfor<2, 5>([&](auto S_c) INL { for (int c = 0; c < 4; ++c) rd4(S_c, c); });
    const int kq = 2 * (rt & 1) + kh;
    u32 fb[2][4];
#pragma unroll
    for (int t = 0; t < 2; ++t)
#pragma unroll
      for (int m = 0; m < 4; ++m) { fb[t][m] = (u32)(WS_F + ((RT2 ^ t ^ 1) * 56 + 14 * kq + 7 * ((m >> 1) ^ swp) + 4 * (m & 1)) * 1024); PINS(fb[t][m]); }
#pragma unroll
    for (int c = 0; c < 8; ++c) acc[c] = f32x4{0.f, 0.f, 0.f, 0.f};
    acc[4] = unpk4(res.x, res.y); acc[5] = unpk4(res.z, res.w);
    const u32 xw = L_RED4 + vlane;
    const u32 x0a = xw + (w ^ 1) * 6144 + 1 * 2048;
    const u32 x0b = xw + (w ^ 5) * 6144 + 2 * 2048;
    const u32 x1b = xw + (w ^ 4) * 6144;
    const u32 oo = (u32)((16 * RT2 + li) * 1024 + (64 * cb + 32 * kh + 16 * (g & 1) + 8 * (g >> 1)) * 2);
    PINV(chk);
    flags_ok(chk, tag, lane >= 24 && lane < 40, vchk, rsW, budget);
    STAMP(13);
    BARRIER();
    sfor<18, 28>([&](auto N_) INL { dma_w2(N_); });
    SB();
    STAMP(14);
    v4u fa[28];
    gate(fa[0], fa[1], (u32)(((lane >> 2) & 1) * 57344 + (3 + 7 * ((lane >> 1) & 1)) * 1024 + 1012 + (lane & 1) * 8), vlane, rsW,
         (u32)(WS_F + ((RT2 & ~1) * 56 + 14 * kq) * 1024), fb[0][0], fb[1][0], budget);
    STAMP(15);
#define FLD(u) ((unit_step(u, 14) % 7) & 3) * 1024
#define FSB(u) fb[unit_tile(u, 14)][2 * (unit_step(u, 14) / 7) + ((unit_step(u, 14) % 7) >> 2)]
    sfor<2, 8>([&](auto U_) INL { constexpr int u = U_.value; ldg<FLD(u)>(fa[u], vlane, rsW, FSB(u)); });
    SB();
    u64 bad = probe<7>(fa[0]);
    sfor<0, 28>([&](auto U_) INL {
      constexpr int u = U_.value, tl = unit_tile(u, 14), s = unit_step(u, 14);
      mend<FLD(u)>(bad, fa[u], vlane, rsW, FSB(u), budget);
      if constexpr (u == 2 || u == 20) BAR_RAW();
      sfor<0, 4>([&](auto C_) INL {
        constexpr int c = C_.value ^ 2;
        acc[4 * tl + c] = mfma32(b4[s & 7][c], fa[u], acc[4 * tl + c]);
        if constexpr (u >= 2 && u < 20 && (C_.value & 1)) rd4(IC<(u >> 1) + 4>{}, 2 * (u & 1) + (C_.value >> 1));
        if constexpr ((u == 25 || u == 26) && (C_.value & 1)) {
          constexpr int i = 2 * (u - 25) + (C_.value >> 1);
          lds_st<f32x4>((i < 2 ? x0a : x0b) + (i & 1) * 1024, acc[i]);
        }
        SB();
        if constexpr (C_.value == 0 && u < 20) ldg<FLD(u + 8)>(fa[u + 8], vlane, rsW, FSB(u + 8));
        if constexpr (C_.value == 2 && u < 27) bad = probe<(u + 1 < 21 ? 7 : 26 - u)>(fa[u + 1]);
        SB();
      });
    });
    STAMP(16);
    lds_st<f32x4>(x1b, acc[6]);
    lds_st<f32x4>(x1b + 1024, acc[7]);
    BARRIER();
    f32x4 k0 = acc[4], k1 = acc[5];
#pragma unroll
    for (int i = 0; i < 3; ++i) {
      k0 += lds_ld<f32x4>(xw + w * 6144 + i * 2048);
      k1 += lds_ld<f32x4>(xw + w * 6144 + i * 2048 + 1024);
    }
    const auto s0 = __builtin_amdgcn_permlane16_swap(pk2(k0[0], k0[1]), pk2(k1[0], k1[1]), false, false);
    const auto s1 = __builtin_amdgcn_permlane16_swap(pk2(k0[2], k0[3]), pk2(k1[2], k1[3]), false, false);
    __builtin_amdgcn_raw_buffer_store_b128(v4u{s0[0], s1[0], s0[1], s1[1]}, rsOut, oo, 0, 0);
    if ((pp & 1) == 0 && cb == 0 && tid == 0) stg2(tag, 0u, rsW, (u32)(WS_EPOCH + 8 * (pp >> 1)));
  }
  STAMP(17);
  asm volatile(".Lfb_end:" ::: "memory");
}

}

extern "C" void kernel_launch(void* const* d_in, const int* in_sizes, int n_in, void* d_out, int out_size, void* d_ws,
                              size_t ws_size, hipStream_t stream) {
  (void)in_sizes; (void)n_in; (void)out_size; (void)ws_size;
  (void)hipFuncSetAttribute((const void*)fused_block, hipFuncAttributeMaxDynamicSharedMemorySize, LDS_BYTES);
  hipLaunchKernelGGL(fused_block, dim3(256), dim3(NTHR), LDS_BYTES, stream, (const u16*)d_in[0], (const u16*)d_in[1],
                     (const u16*)d_in[2], (const u16*)d_in[4], (const u16*)d_in[5], (u16*)d_out, (unsigned char*)d_ws);
}
```

```cpp
#include <hip/hip_runtime.h>
#include <hip/hip_bf16.h>
#include <stdint.h>
#include <type_traits>

namespace {

typedef unsigned int u32;
typedef unsigned long long u64;
typedef unsigned short u16;
typedef u32 v4u __attribute__((ext_vector_type(4)));
typedef u32 v2u __attribute__((ext_vector_type(2)));
typedef u32 v8u __attribute__((ext_vector_type(8)));
typedef float f32x4 __attribute__((ext_vector_type(4)));
typedef float f32x2 __attribute__((ext_vector_type(2)));
typedef short s16x4 __attribute__((ext_vector_type(4)));
typedef short s16x8 __attribute__((ext_vector_type(8)));
typedef __bf16 bf2 __attribute__((ext_vector_type(2)));
typedef __bf16 bf8 __attribute__((ext_vector_type(8)));
#define LAS __attribute__((address_space(3)))
#define INL __attribute__((always_inline))
typedef __amdgpu_buffer_rsrc_t rsrc_t;

constexpr int S_ = 2048, D_ = 512, F_ = 1792;
constexpr int NTHR = 512;
constexpr int RSRC_W3 = 0x00020000;
constexpr float EPS = 1e-5f;
constexpr float LOG2E = 1.44269504088896340736f;

constexpr int WS_O = 0;
constexpr int WS_H = WS_O + 128 * 16 * 1024;
constexpr int WS_F = WS_H + 128 * 16 * 1024;
constexpr int WS_ST = WS_F + 128 * 56 * 1024;
constexpr int WS_EPOCH = WS_ST + 128 * 16 * 128;
constexpr int WS_FF = WS_EPOCH + 16 * 128;
constexpr int WS_FO = WS_FF + 4096 * 128;
constexpr int WS_FH = WS_FO + 256 * 128;
constexpr int WS_END = WS_FH + 256 * 128;

constexpr int LDS_BYTES = 163840;
constexpr int L_XOFF = 24576;
constexpr int L_HALO = 131072;
constexpr int L_SCR = 147456;
constexpr int L_SV = L_SCR;
constexpr int L_SK = L_HALO;
constexpr int L_SQ = 122880;
constexpr int L_ST = L_SCR + 14336;
constexpr int L_CS = L_SCR + 14848;
constexpr int L_RED1 = 155648;
constexpr int L_XRES = L_SCR;
constexpr int L_DUMP1 = L_SCR + 12288;
constexpr int L_DUMP3 = 0;
constexpr int L_CS3 = LDS_BYTES - 512;
constexpr int w1_addr(int s) { return (s < 4 ? 65536 : 8192) + (s & 3) * 7168; }
constexpr int L_X3 = 8192;
constexpr int w2_quad(int s) { return s >= 9 ? w2_quad(s - 9) : 8192 + 16384 * (s < 2 ? 7 + s : s < 5 ? s + 2 : s - 5); }
constexpr int L_RED4 = w2_quad(5);
constexpr int unit_tile(int u, int n) { return u < 2 * n - 6 ? (u & 1) : (u >= 2 * n - 3); }
constexpr int unit_step(int u, int n) { return u < 2 * n - 6 ? (u >> 1) : n - 3 + (u - (2 * n - 6)) % 3; }

constexpr int HW = 6, TU = 10, BU = 10, TAILP = 1, MIDP = 4, ENDP = 3;
constexpr u32 SPIN_CAP = 1u << 17;
constexpr u32 MARK = 0xffffffffu;

template <int I, int N, class F> __device__ __forceinline__ void sfor(F&& f) {
  if constexpr (I < N) { f(std::integral_constant<int, I>{}); sfor<I + 1, N>(f); }
}
template <int V> using IC = std::integral_constant<int, V>;
#define PINV(x) asm volatile("" : "+v"(x))
#define PINS(x) asm volatile("" : "+s"(x))
template <int LO, int SO> __device__ __forceinline__ void dma(u32 lb, u32 vo, rsrc_t rs, u32 sb) {
  u32 t;
  asm volatile("s_add_u32 m0, %1, %5\n\ts_add_u32 %0, %4, %6\n\tbuffer_load_dwordx4 %2, %3, %0 offen lds"
               : "=&s"(t) : "s"(lb), "v"(vo), "s"(rs), "s"(sb), "i"(LO), "i"(SO) : "scc", "memory");
}
template <int O> __device__ __forceinline__ void ldg2(v2u& d, u32 vo, rsrc_t rs, u32 sb) {
  asm volatile("buffer_load_dwordx2 %0, %1, %2, %3 offen offset:%4 sc1" : "=v"(d) : "v"(vo), "s"(rs), "s"(sb), "i"(O) : "memory");
}
template <int O> __device__ __forceinline__ void ldg(v4u& d, u32 vo, rsrc_t rs, u32 sb) {
  asm volatile("buffer_load_dwordx4 %0, %1, %2, %3 offen offset:%4 sc1" : "=v"(d) : "v"(vo), "s"(rs), "s"(sb), "i"(O) : "memory");
}
__device__ __forceinline__ void ldw(v4u& d, u32 vo, rsrc_t rs, u32 sb) {
  asm volatile("buffer_load_dwordx4 %0, %1, %2, %3 offen" : "=v"(d) : "v"(vo), "s"(rs), "s"(sb) : "memory");
}
template <int O> __device__ __forceinline__ void stg(v4u d, u32 vo, rsrc_t rs, u32 sb) {
  asm volatile("buffer_store_dwordx4 %0, %1, %2, %3 offen offset:%4 sc1\n\ts_nop 1" :: "v"(d), "v"(vo), "s"(rs), "s"(sb), "i"(O) : "memory");
}
__device__ __forceinline__ void stg2(v2u d, u32 vo, rsrc_t rs, u32 sb) {
  asm volatile("buffer_store_dwordx2 %0, %1, %2, %3 offen sc1" :: "v"(d), "v"(vo), "s"(rs), "s"(sb) : "memory");
}
template <int L> __device__ __forceinline__ void touch(u64 a) {
  asm volatile("s_mov_b32 m0, %1\n\ts_nop 0\n\tglobal_load_lds_dword %0, off" :: "v"(a), "i"(L) : "memory");
}
__device__ __forceinline__ void pin4(v4u (&a)[4]) { asm volatile("" : "+v"(a[0]), "+v"(a[1]), "+v"(a[2]), "+v"(a[3])); }
template <int N> __device__ __forceinline__ void waitv(v4u& d) { asm volatile("s_waitcnt vmcnt(%1)" : "+v"(d) : "i"(N)); }
template <int N> __device__ __forceinline__ void wait2(v2u& a, v2u& b) {
  asm volatile("s_waitcnt vmcnt(%1)" : "+v"(a) : "i"(N));
  asm volatile("" : "+v"(b));
}
template <int N> __device__ __forceinline__ void waitvm() { asm volatile("s_waitcnt vmcnt(%0)" :: "i"(N) : "memory"); }
#define SB() __builtin_amdgcn_sched_barrier(0)
#define BAR_RAW() do { SB(); asm volatile("" ::: "memory"); __builtin_amdgcn_s_barrier(); asm volatile("" ::: "memory"); SB(); } while (0)
#define BARRIER() do { SB(); asm volatile("s_waitcnt lgkmcnt(0)" ::: "memory"); __builtin_amdgcn_s_barrier(); asm volatile("" ::: "memory"); SB(); } while (0)

__device__ __forceinline__ s16x4 tr4(u32 a) { return __builtin_amdgcn_ds_read_tr16_b64_v4i16((LAS s16x4*)(uintptr_t)a); }
__device__ __forceinline__ s16x8 cat8(s16x4 a, s16x4 b) { return __builtin_shufflevector(a, b, 0, 1, 2, 3, 4, 5, 6, 7); }
template <int T> __device__ __forceinline__ s16x8 trf(u32 a) { return cat8(tr4(a), tr4(a + T)); }
template <class T> __device__ __forceinline__ T lds_ld(u32 a) { return *(LAS T*)(uintptr_t)a; }
template <class T> __device__ __forceinline__ void lds_st(u32 a, T v) { *(LAS T*)(uintptr_t)a = v; }
__device__ __forceinline__ f32x4 mfma32(s16x8 a, v4u b, f32x4 c) {
  return __builtin_amdgcn_mfma_f32_16x16x32_bf16(__builtin_bit_cast(bf8, a), __builtin_bit_cast(bf8, b), c, 0, 0, 0);
}
__device__ __forceinline__ f32x4 mfma16(v2u a, v2u b, f32x4 c) {
  return __builtin_amdgcn_mfma_f32_16x16x16bf16_1k(__builtin_bit_cast(s16x4, a), __builtin_bit_cast(s16x4, b), c, 0, 0, 0);
}
__device__ __forceinline__ u32 pk2(float lo, float hi) {
  bf2 p; p.x = (__bf16)lo; p.y = (__bf16)hi;
  return __builtin_bit_cast(u32, p);
}
__device__ __forceinline__ u32 f2u(float v) { return __builtin_bit_cast(u32, v); }
__device__ __forceinline__ float u2f(u32 v) { return __builtin_bit_cast(float, v); }
__device__ __forceinline__ f32x4 unpk4(u32 a, u32 b) { return f32x4{u2f(a << 16), u2f(a & 0xffff0000u), u2f(b << 16), u2f(b & 0xffff0000u)}; }
__device__ __forceinline__ float max4f(float a, float b, float c, float d) { return __builtin_fmaxf(__builtin_fmaxf(__builtin_fmaxf(a, b), c), d); }
template <int N> struct Base {
  u32 v[N];
  __device__ __forceinline__ void set(u32 a) {
#pragma unroll
    for (int i = 0; i < N; ++i) { v[i] = a + i * 57344; PINV(v[i]); }
  }
  template <int OFF> __device__ __forceinline__ u32 at() const { return v[OFF / 57344] + OFF % 57344; }
};
template <bool MAX> __device__ __forceinline__ float red2(float a, float b) {
  auto r = __builtin_amdgcn_permlane16_swap(f2u(a), f2u(b), false, false);
  u32 r0 = r[0], r1 = r[1];
  float v;
  if constexpr (MAX) asm("v_max_f32 %0, %1, %2" : "=v"(v) : "v"(r0), "v"(r1)); else v = u2f(r0) + u2f(r1);
  r = __builtin_amdgcn_permlane32_swap(f2u(v), f2u(v), false, false);
  r0 = r[0]; r1 = r[1];
  if constexpr (MAX) asm("v_max_f32 %0, %1, %2" : "=v"(v) : "v"(r0), "v"(r1)); else v = u2f(r0) + u2f(r1);
  return v;
}
__device__ __forceinline__ void spread2(float v, float& a, float& b) {
  const auto r = __builtin_amdgcn_permlane16_swap(f2u(v), f2u(v), false, false);
  const u32 r0 = r[0], r1 = r[1];
  a = u2f(r0); b = u2f(r1);
}
__device__ __forceinline__ void ln_stats(float s, float q, float& rstd, float& mean) {
  spread2(red2<false>(s, q) * (1.0f / D_), mean, q);
  rstd = __builtin_amdgcn_rsqf(__builtin_fmaxf(q - mean * mean, 0.0f) + EPS);
}
__device__ __forceinline__ v2u ln_pack(f32x4 a, f32x4 cs, float rstd, float mean) {
  const f32x4 t = (a - mean * cs) * rstd;
  return v2u{pk2(t[0], t[1]), pk2(t[2], t[3])};
}
__device__ __forceinline__ u32 unmark(u32 v) { return min(v, MARK - 1u); }
__device__ __forceinline__ u64 marked(const v4u& a) {
  const u32 x = a.x, y = a.y, z = a.z, w = a.w;
  return __builtin_amdgcn_ballot_w64(max(max(x, y), max(z, w)) == MARK);
}
template <int N> __device__ __forceinline__ u64 probe(v4u& a) {
  asm volatile("s_waitcnt vmcnt(%1)" : "+v"(a) : "i"(N));
  u64 bad = marked(a);
  PINS(bad);
  return bad;
}
template <int O> __device__ __forceinline__ void reload(v4u& a, u32 vl, rsrc_t rs, u32 sb) {
  asm volatile("buffer_load_dwordx4 %0, %1, %2, %3 offen offset:%4 sc1" : "+v"(a) : "v"(vl), "s"(rs), "s"(sb), "i"(O) : "memory");
}
template <int O, class F> __device__ __forceinline__ void mend(u64 bad, v4u& a, u32 vl, rsrc_t rs, u32 sb, u32& budget, F&& again) {
  if (__builtin_expect(bad != 0ull, 0)) {
    do {
      if (budget == 0) break;
      --budget;
      asm volatile("buffer_load_dwordx4 %0, %1, %2, %3 offen offset:%4 sc1\n\ts_waitcnt vmcnt(0)"
                   : "=&v"(a) : "v"(vl), "s"(rs), "s"(sb), "i"(O) : "memory");
    } while (marked(a) != 0ull);
    again();
  }
}
template <int N> __device__ __forceinline__ void take_stats(v2u (&st)[4], u32 vo, rsrc_t rs, u32 sb, u32& budget) {
  asm volatile("s_waitcnt vmcnt(%4)" : "+v"(st[0]), "+v"(st[1]), "+v"(st[2]), "+v"(st[3]) : "i"(N) : "memory");
  auto bad = [&]() INL {
    const u32 m = max(max(max(st[0].x, st[0].y), max(st[1].x, st[1].y)), max(max(st[2].x, st[2].y), max(st[3].x, st[3].y)));
    return __builtin_amdgcn_ballot_w64(m == MARK) != 0ull;
  };
  if (__builtin_expect(bad(), 0)) {
    do {
      if (budget == 0) break;
      --budget;
      asm volatile("buffer_load_dwordx2 %0, %4, %5, %6 offen sc1\n\tbuffer_load_dwordx2 %1, %4, %5, %6 offen offset:128 sc1\n\t"
                   "buffer_load_dwordx2 %2, %4, %5, %6 offen offset:256 sc1\n\tbuffer_load_dwordx2 %3, %4, %5, %6 offen offset:384 sc1\n\t"
                   "s_waitcnt vmcnt(0)" : "=&v"(st[0]), "=&v"(st[1]), "=&v"(st[2]), "=&v"(st[3]) : "v"(vo), "s"(rs), "s"(sb) : "memory");
    } while (bad());
  }
}
__device__ __forceinline__ void flags_ok(v2u& chk, v2u tag, bool use, u32 vo, rsrc_t rs, u32& budget) {
  for (;;) {
    const u32 c0 = chk.x, c1 = chk.y, t0 = tag.x, t1 = tag.y;
    if (__builtin_expect(__builtin_amdgcn_ballot_w64(use && (c0 != t0 || c1 != t1)) == 0ull || budget == 0, 1)) break;
    --budget;
    asm volatile("buffer_load_dwordx2 %0, %1, %2, 0 offen sc1\n\ts_waitcnt vmcnt(0)" : "=&v"(chk) : "v"(vo), "s"(rs) : "memory");
  }
}

__global__ void __launch_bounds__(NTHR, 2)
fused_block(const u16* x_, const u16* wqkv_, const u16* wo_, const u16* w1_, const u16* w2_, u16* out_, unsigned char* ws_) {
  __builtin_amdgcn_s_setprio(3);
  v8u ka0; v4u ka1; v2u ka2;
  asm volatile("s_load_dwordx8 %0, %3, 0x0\n\ts_load_dwordx4 %1, %3, 0x20\n\ts_load_dwordx2 %2, %3, 0x30"
               : "=&s"(ka0), "=&s"(ka1), "=&s"(ka2) : "s"(__builtin_amdgcn_kernarg_segment_ptr()) : "memory");
  const int tid = threadIdx.x, lane = tid & 63, li = lane & 15, g = lane >> 4;
  const int w = __builtin_amdgcn_readfirstlane(tid >> 6);
  const int b = blockIdx.x, xg = b & 7, sl = b >> 3;
  const int pp1 = 8 * (xg >> 1) + (sl >> 2), hq = 4 * (xg & 1) + (sl & 3);
  const int pp = 4 * xg + (sl >> 3), cb = (sl ^ 1) & 7;
  const int p3 = 4 * (xg >> 1) + (sl >> 3), j = 8 * (xg & 1) + (sl & 7);
  const int rt = w & 3, kh = w >> 2;
  const int RT2 = 4 * pp + rt;
  const int T1 = 8 * p3 + 2 * rt + kh;

  const int r8 = lane >> 3, u8 = lane & 7;
  const u32 colsw = (u32)((16 * ((u8 >> 1) ^ ((r8 >> 1) & 3)) + 8 * (u8 & 1)) * 2);
  u32 voB = (u32)((8 * (r8 >> 2) + (r8 & 3)) * 3072) + colsw;
  u32 voX = (u32)(r8 * 1024 + ((u8 ^ g) * 16));
  u32 voXw = voX ^ (u32)((w & 1) * 64);
  u32 lw = (u32)(w * 1024);
  const int rho = xg & 6;
  u32 sbX = (u32)((64 * pp1 + 8 * w) * 1024);
  u32 sbQ = (u32)((32 * (w >> 2) + 16 * (w & 1) + 4 * ((w >> 1) & 1)) * 3072 + hq * 128);
  u32 hr = (u32)(max(64 * pp1 - 16, 0) * 1024 + (w ^ rho) * 128);
  u32 vtag = (u32)(128 * ((g & 1) ? p3 : g == 2 ? (pp1 >> 1) : (pp >> 1)));
  u32 so_o = (u32)(WS_O + ((4 * pp1 + rt) * 16 + 2 * hq + kh) * 1024);
  u32 vlane = (u32)(lane * 16);
  v4u mark4 = {MARK, MARK, MARK, MARK};
  PINS(so_o); PINV(vlane); PINV(mark4);
  PINV(voB); PINV(voX); PINV(voXw); PINV(vtag); PINS(lw); PINS(sbX); PINS(sbQ); PINS(hr);
  const int q4 = (lane >> 2) & 3, p4 = lane & 3;
  const u32 sw64 = (2 * g + (q4 >> 1)) & 3;
  u32 trb = (4 * g + q4) * 128 + 8 * p4;
  Base<3> bqa; bqa.set(trb + 32 * (rt ^ sw64));
  u32 ab[2] = {(u32)(li * 128 + ((g ^ (li >> 1)) * 16)), (u32)(li * 128 + ((g ^ (li >> 1)) * 16)) ^ 64u};
  Base<2> aa[2]; aa[0].set(ab[0] + L_XOFF + kh * 4096); aa[1].set(ab[1] + L_XOFF + kh * 4096);
  Base<2> a3[2];
#pragma unroll
  for (int e = 0; e < 2; ++e) a3[e].set(ab[e] + (kh ? (u32)(L_XOFF + rt * 2048) : (u32)L_HALO));
  v4u ones = {0x3f803f80u, 0x3f803f80u, 0x3f803f80u, 0x3f803f80u};
  PINV(ones);
  SB();
  asm volatile("s_waitcnt lgkmcnt(0)" : "+s"(ka0), "+s"(ka1), "+s"(ka2) :: "memory");
  auto ptr = [](u32 lo, u32 hi) INL { return (void*)(((u64)hi << 32) | lo); };
  void* const x = ptr(ka0[0], ka0[1]); void* const wqkv = ptr(ka0[2], ka0[3]); void* const wo = ptr(ka0[4], ka0[5]);
  void* const w1 = ptr(ka0[6], ka0[7]); void* const w2 = ptr(ka1[0], ka1[1]); void* const out = ptr(ka1[2], ka1[3]);
  unsigned char* const ws = (unsigned char*)ptr(ka2[0], ka2[1]);
  rsrc_t rsX = __builtin_amdgcn_make_buffer_rsrc(x, 0, S_ * D_ * 2, RSRC_W3);
  rsrc_t rsQ = __builtin_amdgcn_make_buffer_rsrc(wqkv, 0, D_ * 3 * D_ * 2, RSRC_W3);
  auto chunk = [&](auto C_, auto I_) INL {
    constexpr int c = C_.value, i = I_.value;
    if constexpr (i == 0) dma<(c & 3) * 32768 + L_XOFF, 0>(lw, voXw, rsX, sbX + (c ^ rho) * 128);
    else dma<(c & 3) * 32768 + (i - 1) * 8192, (i - 1) * 1024>(lw, voB, rsQ, sbQ + (c ^ rho) * 196608);
  };
  rsrc_t rsW = __builtin_amdgcn_make_buffer_rsrc((void*)ws, 0, WS_END, RSRC_W3);
  auto halo = [&]() INL {
    dma<L_HALO, 0>(2 * lw, voX, rsX, hr);
    dma<L_HALO + 1024, 8192>(2 * lw, voX ^ 64u, rsX, hr);
  };
  if (w < 2) halo();
  sfor<0, 4>([&](auto I_) INL { chunk(IC<0>{}, I_); });
  __builtin_amdgcn_s_setprio(2);
  v2u tag;
  ldg2<0>(tag, vtag, rsW, (u32)WS_EPOCH);
  u64 ta;
  {
    const u32 cq = ((3u - (ka0[2] >> 7)) & 3u) + 4 * (xg & 1), co = (3u - (ka0[4] >> 7)) & 7u;
    if (w == 7) ta = lane < 48 ? (u64)wqkv + (u32)((lane * 32 + sl) * 1024 + cq * 128) : (u64)wo + (u32)(((lane - 48) * 32 + sl) * 1024 + co * 128);
    else if (w == 6) ta = (u64)x + (u32)((xg >> 1) * 524288 + ((xg & 1) * 32 + sl) * 8192 + lane * 128);
    else if (w == 5) ta = (u64)w1 + (u32)((4 * ((xg >> 1) * 32 + sl) + g) * 3584 + (xg & 1) * 1792 + min(li, 13) * 128);
    else if (w == 4) ta = (u64)wo + (u32)(b * 2048 + li * 128);
    else ta = (u64)w2 + (u32)(b * 7168 + min(lane, 55) * 128);
  }
  auto touch64 = [&]() INL { touch<L_DUMP1>(ta); };
  if (w >= 6) touch64();
  sfor<0, 4>([&](auto I_) INL { chunk(IC<1>{}, I_); });
  __builtin_amdgcn_s_setprio(0);
  SB();

  rsrc_t rsO = __builtin_amdgcn_make_buffer_rsrc(wo, 0, D_ * D_ * 2, RSRC_W3);
  rsrc_t rs1 = __builtin_amdgcn_make_buffer_rsrc(w1, 0, D_ * F_ * 2, RSRC_W3);
  rsrc_t rs2 = __builtin_amdgcn_make_buffer_rsrc(w2, 0, F_ * D_ * 2, RSRC_W3);
  rsrc_t rsOut = __builtin_amdgcn_make_buffer_rsrc(out, 0, S_ * D_ * 2, RSRC_W3);
  PINS(rsO); PINS(rs1); PINS(rsW);
  u32 budget = SPIN_CAP;
  const int sg = ((sl >> 3) & 2) ^ (2 * (sl & 1)), so_ = ((sl >> 3) & 2) ^ (sl & 1), swp = (sl >> 3) & 1;
  u32 vo64 = (u32)(r8 * 1024) + colsw;
  u32 vo1;
  { const int chi = 64 * w + lane; vo1 = w < 7 ? (u32)((chi / 14) * 3584 + (chi % 14) * 16) : 0u; }
  u32 lw1 = (u32)(w < 7 ? w * 1024 : 98304);
  u32 sb1[4];
#pragma unroll
  for (int i = 0; i < 4; ++i) { sb1[i] = (u32)(224 * j + (i ^ sg) * 458752); PINS(sb1[i]); }
  auto dma_w1 = [&](auto I_) INL {
    constexpr int i = I_.value;
    dma<w1_addr(0) + i * 7168, (i & 3) * 114688>(lw1, vo1, rs1, sb1[i >> 2]);
  };
  auto dma_w1b = [&](auto I_) INL {
    constexpr int i = I_.value;
    dma<w1_addr(4) + i * 7168, (i & 3) * 114688>(w < 7 ? lw : 131072u, vo1, rs1, sb1[3]);
  };
  v4u w1r[4];
  auto ld_w1 = [&](auto I_) INL {
    constexpr int i = I_.value;
    ldw(w1r[i], vo1, rs1, sb1[2] + i * 114688);
  };
  u32 sbO = (u32)(w * 8192 + cb * 128);
  u32 so_h = (u32)(WS_H + (RT2 * 16 + 2 * cb + kh) * 1024), so_st = (u32)(WS_ST + (RT2 * 16 + 2 * cb + kh) * 128);
  u32 so_f = (u32)(WS_F + T1 * 57344 + j * 3584);
  u32 vflag = (u32)((g == 0 ? WS_FH + (pp * 8 + cb) * 128 : WS_FO + (pp1 * 8 + hq) * 128) + li * 8);
  u32 vfo = (u32)(WS_FO + (pp * 8 + min(lane, 7)) * 128);
  PINV(vfo);
  PINV(vflag); PINV(vlane); PINV(vo64); PINV(vo1);
  PINS(lw1); PINS(sbO); PINS(so_o); PINS(so_h); PINS(so_st);
  u32 so_xr = (u32)((64 * pp + 8 * w) * 1024 + cb * 128);
  PINS(so_xr);

  u32 bw[4];
#pragma unroll
  for (int c = 0; c < 4; ++c) bw[c] = trb + 32 * (c ^ (2 * kh) ^ sw64);
  f32x4 bias_own, bias_prev;
  v4u pmask;
  {
    const int par = li & 1;
    const bool ownA = (4 * g + par) <= li, ownB = (4 * g + par + 2) <= li, first = (pp1 == 0) && (rt == 0);
#pragma unroll
    for (int r = 0; r < 4; ++r) {
      const bool own = (r & 2) ? ownB : ownA, ok = (r & 1) == par;
      bias_own[r] = (ok && own) ? 0.f : -1e30f;
      bias_prev[r] = (ok && !own && !first) ? 0.f : -1e30f;
    }
    const u32 hm = par ? 0xffff0000u : 0x0000ffffu;
    pmask = v4u{ownA ? 0u : hm, ownB ? 0u : hm, ownA ? hm : 0u, ownB ? hm : 0u};
  }
  PINV(bias_own); PINV(bias_prev); PINV(pmask);
  u32 a_st = (u32)(L_ST + li * 8), a_cs = (u32)(L_CS + rt * 192 + g * 16), a_vw = (u32)(L_SV + rt * 2560 + li * 32 + g * 8);
  u32 a_kw = (u32)(L_SK + rt * 2560 + lane * 8), a_qw = (u32)(L_SQ + rt * 2048 + kh * 1024 + lane * 8);
  u32 a_kr = (u32)(L_SK + kh * 5120 + rt * 512 + lane * 8), a_qr = (u32)(L_SQ + kh * 4096 + rt * 512 + lane * 8);
  u32 a_xr = (u32)(L_XRES + rt * 2048 + li * 128 + (g & 1) * 8 + (((4 * kh + (g >> 1)) ^ (li >> 1)) * 16));
  PINV(a_xr); PINV(a_st); PINV(a_cs); PINV(a_vw); PINV(a_kw); PINV(a_qw); PINV(a_kr); PINV(a_qr);

  SB();
  f32x4 acc[14];
#pragma unroll
  for (int c = 0; c < 10; ++c) acc[c] = f32x4{0.f, 0.f, 0.f, 0.f};
  v4u res;
  v2u chk;
    s16x8 fb[2][3];
    v4u fa[2][3];
    float rsh, mnh;
    auto rd = [&](auto K_, auto H_, auto N_) INL {
      constexpr int H = H_.value, N = N_.value, SL = ((H >> 1) & 3) * 32768, e = H & 1;
      if constexpr (N < 3) fb[e][N] = trf<2048>(bqa.at<SL + N * 8192 + e * 4096>());
      else if constexpr (N < 5) fa[e][N - 3] = lds_ld<v4u>(aa[e].template at<SL + (N - 3) * 2048>());
      else fa[e][2] = lds_ld<v4u>(a3[e].template at<(K_.value ? SL : (H >> 1) * 2048)>());
    };
    auto mm = [&](auto K_, int e, auto N_) INL {
      constexpr int N = N_.value;
      if constexpr (N < 6) acc[N] = mfma32(fb[e][N % 3], fa[e][N / 3], acc[N]);
      else if constexpr (N == 6) acc[9] = mfma32(__builtin_bit_cast(s16x8, fa[e][2]), fa[e][2], acc[9]);
      else if constexpr (K_.value == 0) acc[N - 1] = mfma32(fb[e][N - 7], fa[e][2], acc[N - 1]);
      else acc[6] = mfma32(__builtin_bit_cast(s16x8, ones), fa[e][2], acc[6]);
    };
    if (w >= 6) waitvm<5>(); else waitvm<4>();
    PINV(tag);
    if (w < 2 && lane < 8) lds_st<v4u>(L_HALO + 2 * lw + vlane, ones);
    BARRIER();
    auto first = [&](auto K_) INL {
      sfor<0, 6>([&](auto N_) INL { rd(K_, IC<0>{}, N_); });
      SB();
      sfor<0, (K_.value ? 16 : 20)>([&](auto P_) INL {
        constexpr int NM = K_.value ? 8 : 10, P = P_.value;
        mm(K_, P / NM, IC<P % NM>{});
        if constexpr (P < 6) rd(K_, IC<1>{}, P_);
        SB();
        if constexpr (P == 2) { if (w >= 2) halo(); SB(); }
        if constexpr (P == 6) { stg<0>(mark4, vlane, rsW, so_o); SB(); }
        if constexpr (P >= 8 && P < 16) { chunk(IC<2 + ((P - 8) >> 2)>{}, IC<(P & 3)>{}); SB(); }
      });
    };
    if (kh) first(IC<1>{}); else first(IC<0>{});
  {
    auto stream = [&](auto C_, auto I_) INL {
      constexpr int C = C_.value, i = I_.value;
      if constexpr (i < 4) {
        if constexpr (C <= 4) chunk(IC<C + 3>{}, I_);
        else if constexpr (C <= 6) dma<(C - 5) * 32768 + i * 8192, (i & 1) * 65536>(lw, vo64, rsO, sbO + ((2 * (C - 5) + (i >> 1)) ^ so_) * 131072);
        else dma_w1(I_);
      }
      if constexpr (i == 4 && C == 1) { if (w >= 4 && w < 6) touch64(); }
      if constexpr (i == 4 && C == 6) { if (w == 3) touch64(); }
      if constexpr (i == 4 && C == 3) dma<L_XRES, 0>(lw, voXw, rsX, so_xr);
    };
    SB();
    if (w < 2) waitvm<9>(); else waitvm<11>();
    { const u64 t = (((u64)tag.y << 32) | tag.x) + 1ull; tag = v2u{(u32)t, (u32)(t >> 32)}; }
    BAR_RAW();
    auto loop = [&](auto K_) INL {
      constexpr int K = K_.value, NM = K ? 8 : 10;
      sfor<0, 6>([&](auto N_) INL { rd(K_, IC<2>{}, N_); });
      SB();
      sfor<2, 16>([&](auto H_) INL {
        constexpr int H = H_.value, e = H & 1, C = (H + 1) >> 1;
        if constexpr (e && C > 1 && C < 8) {
          waitvm<(C == 4 || C == 5 ? 9 : 8)>();
          if constexpr (C == 2) { if (lane < 8) lds_st<v4u>(L_HALO + 2 * lw + vlane, ones); }
          BARRIER();
          if constexpr (C == 2 && K == 1) { if (w == 4 && g == 2) stg2(tag, vflag, rsW, 0u); }
        }
        sfor<0, NM>([&](auto P_) INL {
          constexpr int P = P_.value, N = H < 15 ? P : (P + 6) % NM;
          mm(K_, e, IC<N>{});
          if constexpr (H < 15 && P < 6) rd(K_, IC<H + 1>{}, P_);
          SB();
          constexpr int R = K ? (P % 2 == 1 && P < 6 ? P / 2 : -1) : (P % 2 == 0 && P >= 4 ? P / 2 - 2 : -1);
          if constexpr (H == 2 && P < 5) { stream(IC<1>{}, P_); SB(); }
          if constexpr (H >= 3 && H < 15 && R >= 0) { stream(IC<C>{}, IC<(e ? 0 : 3) + R>{}); SB(); }
          if constexpr (H == 15 && N == 1) {
            const float dg = (li & 2) ? ((li & 1) ? acc[9][3] : acc[9][2]) : ((li & 1) ? acc[9][1] : acc[9][0]);
            ln_stats(g == 0 ? (K ? acc[6][0] : acc[9][0]) : 0.f, g == (li >> 2) ? dg : 0.f, rsh, mnh);
            SB();
          }
          if constexpr (H == 15 && N == 3) {
            if constexpr (K) {
              if (g == 0) lds_st<f32x2>(a_st + rt * 128, f32x2{rsh, mnh});
            } else {
              if (li == 0) {
#pragma unroll
                for (int m = 0; m < 3; ++m) lds_st<f32x4>(a_cs + m * 64, acc[6 + m]);
              }
            }
            SB();
          }
        });
        if constexpr (H == 13) {
          const v2u r0 = lds_ld<v2u>(a_xr), r1 = lds_ld<v2u>(a_xr ^ 32u);
          res = v4u{r0.x, r0.y, r1.x, r1.y};
          SB();
        }
      });
    };
    if (kh) { __builtin_amdgcn_s_setprio(1); loop(IC<1>{}); } else loop(IC<0>{});
    BARRIER();
    const f32x2 sta = lds_ld<f32x2>(a_st + kh * 256), stb = lds_ld<f32x2>(a_st + kh * 256 + 128);
    const f32x4 csv = lds_ld<f32x4>(a_cs + 128), csk = lds_ld<f32x4>(a_cs + 64), csq = lds_ld<f32x4>(a_cs);
    SB();
    __builtin_amdgcn_s_setprio(2);
    ldg2<0>(chk, vfo, rsW, 0u);
    {
      stg<0>(mark4, vlane, rsW, so_h);
      stg<0>(mark4, vlane & 127u, rsW, so_st);
    }
    dma_w1(IC<4>{}); dma_w1(IC<5>{}); ld_w1(IC<0>{});
    SB();
    lds_st<v2u>(a_qw, ln_pack(acc[0], csq, sta.x, sta.y));
    lds_st<v2u>(a_qw + 512, ln_pack(acc[3], csq, stb.x, stb.y));
    lds_st<v2u>(a_kw + kh * 1024 + 512, ln_pack(acc[1], csk, sta.x, sta.y));
    lds_st<v2u>(a_kw + kh * 1024 + 1024, ln_pack(acc[4], csk, stb.x, stb.y));
    lds_st<v2u>(a_vw + kh * 1024 + 512, ln_pack(acc[2], csv, sta.x, sta.y));
    lds_st<v2u>(a_vw + kh * 1024 + 1024, ln_pack(acc[5], csv, stb.x, stb.y));
    if (kh == 0) {
      lds_st<v2u>(a_kw, ln_pack(acc[7], csk, rsh, mnh));
      lds_st<v2u>(a_vw, ln_pack(acc[8], csv, rsh, mnh));
    }
    SB();
    ld_w1(IC<1>{});
    waitvm<11>();
    BARRIER();
    const v2u qa = lds_ld<v2u>(a_qr), kpa = lds_ld<v2u>(a_kr), ka = lds_ld<v2u>(a_kr + 512);
    const v2u qb = lds_ld<v2u>(a_qr + 2048), kpb = lds_ld<v2u>(a_kr + 2560), kb = lds_ld<v2u>(a_kr + 3072);
    const s16x8 vta = trf<512>(a_kr + (L_SV - L_SK)), vtb = trf<512>(a_kr + (L_SV - L_SK) + 2560);
    SB();
    ld_w1(IC<2>{}); ld_w1(IC<3>{});
    SB();
    const f32x4 s0a = mfma16(kpa, qa, bias_prev), s1a = mfma16(ka, qa, bias_own);
    const f32x4 s0b = mfma16(kpb, qb, bias_prev), s1b = mfma16(kb, qb, bias_own);
    const float aA = max4f(s0a[0], s0a[1], s1a[0], s1a[1]), aB = max4f(s0a[2], s0a[3], s1a[2], s1a[3]);
    const float bA = max4f(s0b[0], s0b[1], s1b[0], s1b[1]), bB = max4f(s0b[2], s0b[3], s1b[2], s1b[3]);
    const float c2 = 0.25f * LOG2E;
    float ma, mb;
    spread2(red2<true>(__builtin_fmaxf(aA, aB), __builtin_fmaxf(bA, bB)) * c2, ma, mb);
    const float eaA = __builtin_amdgcn_exp2f(aA * c2 - ma), eaB = __builtin_amdgcn_exp2f(aB * c2 - ma);
    const float ebA = __builtin_amdgcn_exp2f(bA * c2 - mb), ebB = __builtin_amdgcn_exp2f(bB * c2 - mb);
    float ia, ib;
    spread2(__builtin_amdgcn_rcpf(red2<false>(eaA + eaB, ebA + ebB)), ia, ib);
    const u32 paA = pk2(eaA * ia, eaA * ia), paB = pk2(eaB * ia, eaB * ia), pbA = pk2(ebA * ib, ebA * ib), pbB = pk2(ebB * ib, ebB * ib);
    const f32x4 z = {0.f, 0.f, 0.f, 0.f};
    const f32x4 oa_ = mfma32(vta, v4u{paA, paB, paA, paB} & pmask, z), ob_ = mfma32(vtb, v4u{pbA, pbB, pbA, pbB} & pmask, z);
    stg<0>(v4u{unmark(pk2(oa_[0], oa_[1])), unmark(pk2(oa_[2], oa_[3])), unmark(pk2(ob_[0], ob_[1])), unmark(pk2(ob_[2], ob_[3]))}, vlane, rsW, so_o);
  }
  SB();

  const int jx = (2 * swp) ^ (cb & 3);
  const u32 sb2 = (u32)(448 * (w >> 1) * 1024 + cb * 128);
  auto dma_w2 = [&](auto N_) INL {
    constexpr int n = N_.value, s = n >> 1, jj = s < 8 ? s >> 1 : s - 8, q = s < 8 ? s & 1 : 2;
    if constexpr (s < 12) dma<w2_quad(s) + (n & 1) * 1024, (32 * q + 8 * (n & 1)) * 1024>(2 * lw, vo64, rs2, sb2 + ((jj ^ jx) * 112 + 16 * (w & 1)) * 1024);
    else dma<w2_quad(s) + (n & 1) * 1024, (96 + 8 * (n & 1)) * 1024>(2 * lw, vo64, rs2, sb2 + ((2 * (s - 12) + (w & 1)) ^ jx) * 114688);
  };
  u32 vfh;
  const u32 wb = (4 * g + q4) * 224 + 8 * p4;
  Base<3> ca;
  f32x4 csum = {0.f, 0.f, 0.f, 0.f};
  s16x8 ct[6];
  {
    s16x8 bo[8][4];
    u32 bk[4];
#pragma unroll
    for (int c = 0; c < 4; ++c) bk[c] = bw[c] + kh * 32768;
    sfor<0, 8>([&](auto S_c) INL {
      constexpr int s = S_c.value;
      if constexpr (s < 2) dma_w1(IC<s + 6>{});
      if constexpr (s == 6) {
        waitvm<3>();
        __builtin_amdgcn_s_setprio(0);
      }
#pragma unroll
      for (int c = 0; c < 4; ++c) bo[s][c] = trf<2048>(bk[c] + s * 4096);
      SB();
    });
    acc[0] = unpk4(res.x, res.y); acc[1] = unpk4(res.z, res.w);
    acc[2] = acc[3] = f32x4{0.f, 0.f, 0.f, 0.f};
    const u32 ob[2] = {(u32)(WS_O + (RT2 * 16 + 4 * ((2 * kh) ^ so_)) * 1024), (u32)(WS_O + (RT2 * 16 + 4 * ((2 * kh + 1) ^ so_)) * 1024)};
    const u32 xw = (kh ? L_RED1 : 0) + rt * 2048 + vlane, xr = (kh ? 0 : L_RED1) + rt * 2048 + vlane;
    BARRIER();
    if (w == 7 && g == 0) stg2(tag, vflag, rsW, 0u);
    auto put_w1 = [&]() INL {
      const u32 a_w1 = w1_addr(4) + lw + vlane;
      pin4(w1r);
      if (w < 7) {
#pragma unroll
        for (int i = 0; i < 4; ++i) lds_st<v4u>(a_w1 + i * 7168, w1r[i]);
      }
      SB();
    };
    put_w1();
    PINV(chk);
    flags_ok(chk, tag, lane < 8, vfo, rsW, budget);
    __builtin_amdgcn_s_sleep(2);
    SB();
    v4u oa[8];
    vfh = (u32)(WS_FH + ((T1 >> 2) * 8 + min(max(lane - 16, 0), 7)) * 128);
    sfor<0, 8>([&](auto S_c) INL { ldg<(S_c.value & 3) * 1024>(oa[S_c.value], vlane, rsW, ob[S_c.value >> 2]); });
    SB();
    ca.set(wb + 32 * min(w, 6));
    sfor<0, 6>([&](auto I_) INL { ct[I_.value] = trf<3584>(ca.at<65536 + I_.value * 7168>()); });
    SB();
#pragma unroll
    for (int i = 0; i < 6; ++i) csum = mfma32(ct[i], ones, csum);
    SB();
    sfor<0, 8>([&](auto S_c) INL {
      constexpr int s = S_c.value;
      if constexpr (s >= 4) dma_w1b(S_c);
      mend<(s & 3) * 1024>(probe<(s < 4 ? 7 - s : 4)>(oa[s]), oa[s], vlane, rsW, ob[s >> 2], budget, [&]() INL {
        sfor<s + 1, 8>([&](auto T_) INL { reload<(T_.value & 3) * 1024>(oa[T_.value], vlane, rsW, ob[T_.value >> 2]); });
        waitvm<0>();
      });
#pragma unroll
      for (int c = 0; c < 4; ++c) acc[c ^ 2] = mfma32(bo[s][c ^ 2], oa[s], acc[c ^ 2]);
      SB();
    });
    ldg2<0>(chk, vfh, rsW, 0u);
    lds_st<f32x4>(xw, acc[2]);
    lds_st<f32x4>(xw + 1024, acc[3]);
    BARRIER();
    const f32x4 k0 = acc[0] + lds_ld<f32x4>(xr), k1 = acc[1] + lds_ld<f32x4>(xr + 1024);
    res = v4u{unmark(pk2(k0[0], k0[1])), unmark(pk2(k0[2], k0[3])), unmark(pk2(k1[0], k1[1])), unmark(pk2(k1[2], k1[3]))};
    stg<0>(res, vlane, rsW, so_h);
    SB();
    const f32x4 h0 = unpk4(res.x, res.y), h1 = unpk4(res.z, res.w);
    const f32x4 hs = h0 + h1, hq2 = h0 * h0 + h1 * h1;
    const float sq = red2<false>((hs[0] + hs[1]) + (hs[2] + hs[3]), (hq2[0] + hq2[1]) + (hq2[2] + hq2[3]));
    if (lane < 32)
      asm volatile("buffer_store_dword %0, %1, %2, %3 offen sc1" :: "v"(unmark(f2u(sq))), "v"((u32)(li * 8 + g * 4)), "s"(rsW), "s"(so_st) : "memory");
  }
  SB();

  s16x8 b4[8][4];
  Base<3> bb[4];
  auto rd4 = [&](auto S_c, int c) INL { b4[S_c.value & 7][c] = trf<2048>(bb[c].template at<w2_quad(S_c.value)>()); };
  const int kq = 2 * (rt & 1) + kh;
  const u32 vff = (u32)(WS_FF + (((RT2 & ~1) + (min(lane, 7) >> 2)) * 16 + 4 * kq + (min(lane, 7) & 3)) * 128);
  const u32 so_ff = (u32)(WS_FF + (T1 * 16 + j - 1) * 128);
  {
    Base<2> wbk; wbk.set(wb + kh * 28672);
    s16x8 bq[8][7];
    auto rd3 = [&](auto S_c, auto C_) INL { bq[S_c.value][C_.value] = trf<3584>(wbk.at<w1_addr(S_c.value) + 32 * C_.value>()); };
    sfor<0, 16>([&](auto I_) INL {
      if constexpr ((I_.value & 3) == 0) dma_w2(IC<(I_.value >> 2)>{});
      rd3(IC<(I_.value >> 2)>{}, IC<(I_.value & 3)>{});
    });
    sfor<0, 6>([&](auto I_) INL { ct[I_.value] = trf<3584>(ca.at<(I_.value < 2 ? 65536 + (6 + I_.value) * 7168 : 8192 + (I_.value - 2) * 7168)>()); });
    v4u ha[16];
    v2u st[4];
    u32 hb[2][2];
#pragma unroll
    for (int t = 0; t < 2; ++t)
#pragma unroll
      for (int m = 0; m < 2; ++m) { hb[t][m] = (u32)(WS_H + ((T1 ^ t ^ 1) * 16 + 4 * ((2 * m + kh) ^ sg)) * 1024); PINS(hb[t][m]); }
    SB();
#pragma unroll
    for (int i = 0; i < 6; ++i) csum = mfma32(ct[i], ones, csum);
#pragma unroll
    for (int c = 0; c < 8; ++c) acc[c] = f32x4{0.f, 0.f, 0.f, 0.f};
    const u32 vst = (u32)(g * 512 + li * 8), sst = (u32)(WS_ST + T1 * 2048);
    const u32 xw = L_X3 + 4 * lw + vlane, xr = L_X3 + 4 * (lw ^ 4096u) + vlane, cr_ = L_CS3 + g * 16;
    u32 cr = cr_; PINV(cr);
    const u32 yw = L_X3 + 32768 + 3 * lw + vlane, yr = L_X3 + 32768 + 3 * (lw ^ 4096u) + vlane;
    SB();
    wait2<6>(chk, chk);
    flags_ok(chk, tag, g == 1, vfh, rsW, budget);
    BAR_RAW();
#define HLD(u) (((u) >> 1) & 3) * 1024
#define HSB(u) hb[(u) & 1][(u) >> 3]
    sfor<0, 4>([&](auto I_) INL { ct[I_.value] = trf<3584>(ca.at<8192 + (4 + I_.value) * 7168>()); });
    sfor<16, 16 + 8>([&](auto I_) INL { rd3(IC<(I_.value >> 2)>{}, IC<(I_.value & 3)>{}); });
    SB();
#pragma unroll
    for (int i = 0; i < 4; ++i) csum = mfma32(ct[i], ones, csum);
    SB();
    sfor<0, HW>([&](auto U_) INL { constexpr int u = U_.value; ldg<HLD(u)>(ha[u], vlane, rsW, HSB(u)); });
    SB();
    sfor<16 + 8, 32>([&](auto I_) INL { rd3(IC<(I_.value >> 2)>{}, IC<(I_.value & 3)>{}); });
    if (li == 0 && w < 7) lds_st<f32x4>(L_CS3 + w * 64 + g * 16, csum);
    SB();
    constexpr auto nops = [](int t) { return ((t == 2 || t == 3) ? 2 : 0) + (t < 16 - HW ? 1 : 0) + (t == TU ? 1 : 0); };
    constexpr auto younger = [nops](int uu) {
      int n = uu < HW ? HW - 1 - uu : nops(uu - HW) - 1;
      for (int t = (uu < HW ? 0 : uu - HW + 1); t < uu; ++t) n += nops(t);
      return n;
    };
    u64 bad = probe<HW - 1>(ha[0]);
    float rstd, mean, c1;
    f32x4 cs[4];
    sfor<0, 16>([&](auto U_) INL {
      constexpr int u = U_.value, tl = u & 1, s = u >> 1;
      mend<HLD(u)>(bad, ha[u], vlane, rsW, HSB(u), budget, [&]() INL {
        sfor<u + 1, (u + HW < 16 ? u + HW : 16)>([&](auto T_) INL { reload<HLD(T_.value)>(ha[T_.value], vlane, rsW, HSB(T_.value)); });
        waitvm<0>();
      });
      if constexpr (u == BU) {
        asm volatile("s_waitcnt lgkmcnt(8)" ::: "memory");
        BAR_RAW();
      }
      if constexpr (u == 15) BARRIER();
      sfor<0, 4>([&](auto C_) INL {
        constexpr int c = C_.value;
        acc[4 * tl + c] = mfma32(bq[s][c], ha[u], acc[4 * tl + c]);
        if constexpr (u >= 3 && u < 15 && (c & 1)) rd3(IC<(2 * (u - 3) + c / 2) / 3>{}, IC<4 + (2 * (u - 3) + c / 2) % 3>{});
        if constexpr (u == 15) lds_st<f32x4>(xw + c * 1024, acc[c]);
        if constexpr (u == 13) cs[c] = lds_ld<f32x4>(cr + c * 64);
        if constexpr (u == 14) {
#pragma unroll
          for (int r = 0; r < 4; ++r) { float t = __builtin_fmaf(mean, cs[c][r], acc[4 + c][r]); PINV(t); acc[4 + c][r] = t; }
        }
        SB();
        if constexpr (c == 0 && u < 16 - HW) ldg<HLD(u + HW)>(ha[u + HW], vlane, rsW, HSB(u + HW));
        if constexpr (c == 1 && (u == 2 || u == 3)) {
          ldg2<(2 * (u - 2)) * 128>(st[2 * (u - 2)], vst, rsW, sst);
          ldg2<(2 * (u - 2) + 1) * 128>(st[2 * (u - 2) + 1], vst, rsW, sst);
        }
        if constexpr (u == TU && c == 0) touch<L_DUMP3>((u64)w2 + (w < 7 ? (u32)((w * 32 + sl) * 8192) + (vlane << 3) : 0u));
        if constexpr (u == 12 && c == 0) take_stats<1 + (16 - HW > 4 ? 12 - HW : 0)>(st, vst, rsW, sst, budget);
        if constexpr (u == 12 && c == 1) {
          const float s_ = (u2f(st[0].x) + u2f(st[1].x)) + (u2f(st[2].x) + u2f(st[3].x));
          float q_ = (u2f(st[0].y) + u2f(st[1].y));
          PINV(q_);
          q_ += u2f(st[2].y) + u2f(st[3].y);
          ln_stats(s_, q_, rstd, mean);
          c1 = -LOG2E * rstd; mean = -mean;
          PINV(c1); PINV(mean);
        }
        if constexpr (c == 2 && u < 15) bad = probe<younger(u + 1)>(ha[u + 1]);
        SB();
      });
    });
    f32x4 part[4];
    float v[4], d[4];
    u32 fw[8];
    sfor<0, 48>([&](auto M_) INL {
      constexpr int m = M_.value, tl = m / 24, s = (m % 24) / 3, c = m % 3;
      if constexpr (m < 3) acc[8 + c] = mfma32(bq[s][4 + c], ha[2 * s + tl], f32x4{0.f, 0.f, 0.f, 0.f});
      else acc[8 + 3 * tl + c] = mfma32(bq[s][4 + c], ha[2 * s + tl], acc[8 + 3 * tl + c]);
      if constexpr (m == 2) BARRIER();
      if constexpr (m % 7 == 2 && m < 28) part[m / 7] = lds_ld<f32x4>(xr + (m / 7) * 1024);
      if constexpr (m >= 5 && m < 33) {
        constexpr int cc = (m - 5) / 7, sg_ = (m - 5) % 7;
#pragma unroll
        for (int r = 0; r < 4; ++r) {
          if constexpr (sg_ == 0) v[r] = acc[4 + cc][r] + part[cc][r];
          if constexpr (sg_ == 1) v[r] *= c1;
          if constexpr (sg_ == 2) d[r] = __builtin_amdgcn_exp2f(v[r]);
          if constexpr (sg_ == 3) d[r] = __builtin_fmaf(d[r], -LOG2E, -LOG2E);
          if constexpr (sg_ == 4) d[r] = __builtin_amdgcn_rcpf(d[r]);
          if constexpr (sg_ == 5) v[r] *= d[r];
          if constexpr (sg_ < 2 || sg_ == 5) PINV(v[r]); else if constexpr (sg_ < 5) PINV(d[r]);
        }
        if constexpr (sg_ == 6) { fw[2 * cc] = pk2(v[0], v[1]); fw[2 * cc + 1] = pk2(v[2], v[3]); PINV(fw[2 * cc]); PINV(fw[2 * cc + 1]); }
      }
      if constexpr (m == 8 || m == 15 || m == 20) cs[0] = lds_ld<f32x4>(cr + (m == 8 ? 4 : m == 15 ? 5 : 6) * 64);
      if constexpr (m == 11 || m == 18 || m == 23) {
#pragma unroll
        for (int r = 0; r < 4; ++r) { float t = mean * cs[0][r]; PINV(t); acc[m == 11 ? 11 : m == 18 ? 12 : 13][r] = t; }
      }
      if constexpr (m >= 28 && m < 31) lds_st<f32x4>(yw + (m - 28) * 1024, acc[8 + m - 28]);
      if constexpr (m >= 40) rd4(IC<(m - 40) / 4>{}, (m - 40) % 4);
      SB();
      if constexpr (m == 18 || m == 32) stg<(m / 32) * 1024>(v4u{fw[m / 32 * 4], fw[m / 32 * 4 + 1], fw[m / 32 * 4 + 2], fw[m / 32 * 4 + 3]}, vlane, rsW, so_f);
      if constexpr (m >= 4 && m < 28 && m % 4 == 0) dma_w2(IC<3 + m / 4>{});
      if constexpr (m == 35) {
        BARRIER();
#pragma unroll
        for (int i = 0; i < 3; ++i) part[i] = lds_ld<f32x4>(yr + i * 1024);
      }
      if constexpr (m >= 36 && m < 40) bb[m - 36].set((bw[0] ^ (u32)(32 * (m - 36))) + kq * 4096);
      if constexpr (m >= 37 && m < 45 && (m & 1)) dma_w2(IC<10 + (m - 37) / 2>{});
      SB();
    });
    __builtin_amdgcn_s_setprio(2);
    waitvm<5>();
    BARRIER();
    auto silu = [&](float t) INL { return t * __builtin_amdgcn_rcpf(__builtin_fmaf(__builtin_amdgcn_exp2f(t), -LOG2E, -LOG2E)); };
    sfor<0, 3>([&](auto C_) INL {
      constexpr int c = C_.value;
      sfor<14 + c * TAILP, 14 + (c + 1) * TAILP>([&](auto N_) INL { if constexpr (N_.value < 22) dma_w2(N_); });
      for (int i = 0; i < 4; ++i) rd4(IC<2 + c>{}, i);
      SB();
      const f32x4 t = (acc[11 + c] + part[c]) * c1;
      fw[2 * c] = pk2(silu(t[0]), silu(t[1]));
      fw[2 * c + 1] = pk2(silu(t[2]), silu(t[3]));
      if constexpr (c == 1) {
        SB();
        waitvm<4 + (c + 1) * TAILP>();
        if (g == 1) stg2(tag, vlane >> 1, rsW, so_ff);
      }
      if constexpr (c == 1) stg<2048>(v4u{fw[0], fw[1], fw[2], fw[3]}, vlane, rsW, so_f);
    });
    stg2(v2u{fw[4], fw[5]}, vlane >> 1, rsW, so_f + 3072);
  }
  SB();

  {
    constexpr int N0 = 14 + 3 * TAILP < 22 ? 14 + 3 * TAILP : 22, N1 = N0 + MIDP, N2 = 28 - ENDP;
    BARRIER();
    sfor<N0, N1>([&](auto N_) INL { dma_w2(N_); });
    waitvm<MIDP>();
    if (g == 1) stg2(tag, vlane >> 1, rsW, so_ff + 262144);
    __builtin_amdgcn_s_setprio(1);
    sfor<N1, N2>([&](auto N_) INL { dma_w2(N_); });
    ldg2<0>(chk, vff, rsW, 0u);
    sfor<N2, 28>([&](auto N_) INL { dma_w2(N_); });
    __builtin_amdgcn_s_setprio(0);
    u32 fb[2][4];
#pragma unroll
    for (int t = 0; t < 2; ++t)
#pragma unroll
      for (int m = 0; m < 4; ++m) { fb[t][m] = (u32)(WS_F + (RT2 ^ t ^ 1) * 57344 + (4 * kq + (m ^ jx)) * 3584); PINS(fb[t][m]); }
#pragma unroll
    for (int c = 0; c < 8; ++c) acc[c] = f32x4{0.f, 0.f, 0.f, 0.f};
    acc[4] = unpk4(res.x, res.y); acc[5] = unpk4(res.z, res.w);
    const u32 xw = L_RED4 + vlane;
    const u32 x0a = xw + (w ^ 1) * 6144 + 1 * 2048;
    const u32 x0b = xw + (w ^ 5) * 6144 + 2 * 2048;
    const u32 x1b = xw + (w ^ 4) * 6144;
    const u32 oo = (u32)((16 * RT2 + li) * 1024 + (64 * cb + 32 * kh + 16 * (g & 1) + 8 * (g >> 1)) * 2);
    const u32 vl8 = vlane >> 1;
    SB();
    wait2<ENDP>(chk, chk);
    flags_ok(chk, tag, lane < 8, vff, rsW, budget);
    SB();
    v4u fa[28];
    v2u fh[4][2];
    constexpr int FW = 6;
    auto lda = [&](auto U_) INL {
      constexpr int u = U_.value, tl = unit_tile(u, 14), s = unit_step(u, 14);
      if constexpr (s < 12) ldg<(s < 8 ? s & 1 : 2) * 1024>(fa[u], vlane, rsW, fb[tl][s < 8 ? s >> 1 : s - 8]);
      else { ldg2<3072>(fh[2 * tl + s - 12][0], vl8, rsW, fb[tl][2 * (s - 12)]); ldg2<3072>(fh[2 * tl + s - 12][1], vl8, rsW, fb[tl][2 * (s - 12) + 1]); }
    };
    sfor<0, FW>([&](auto U_) INL { lda(U_); });
    SB();
    constexpr int TF = 16 - 2 * FW;
    static_assert(TF >= 0, "window");
    constexpr auto younger = [](int uu) {
      int n = (uu > TF && uu <= TF + FW) ? 1 : 0;
      for (int t = uu + 1; t < uu + FW && t < 28; ++t) n += unit_step(t, 14) < 12 ? 1 : 2;
      return n;
    };
    sfor<0, 28>([&](auto U_) INL {
      constexpr int u = U_.value, tl = unit_tile(u, 14), s = unit_step(u, 14);
      if constexpr (s < 12) waitv<younger(u)>(fa[u]);
      else {
        wait2<younger(u)>(fh[2 * tl + s - 12][0], fh[2 * tl + s - 12][1]);
        fa[u] = v4u{fh[2 * tl + s - 12][0].x, fh[2 * tl + s - 12][0].y, fh[2 * tl + s - 12][1].x, fh[2 * tl + s - 12][1].y};
      }
      if constexpr (u == TF + FW) {
        wait2<younger(u) - 1>(chk, chk);
        flags_ok(chk, tag, lane < 8, vff + 262144, rsW, budget);
      }
      if constexpr (u == 4 || u == 20) BAR_RAW();
      sfor<0, 4>([&](auto C_) INL {
        constexpr int c = C_.value ^ 2;
        acc[4 * tl + c] = mfma32(b4[s & 7][c], fa[u], acc[4 * tl + c]);
        if constexpr (u >= 4 && u < 22 && (C_.value & 1)) rd4(IC<(u >> 1) + 3>{}, 2 * (u & 1) + (C_.value >> 1));
        if constexpr ((u == 25 || u == 26) && (C_.value & 1)) {
          constexpr int i = 2 * (u - 25) + (C_.value >> 1);
          lds_st<f32x4>((i < 2 ? x0a : x0b) + (i & 1) * 1024, acc[i]);
        }
        SB();
        if constexpr (C_.value == 0 && u + FW < 28) lda(IC<u + FW>{});
        if constexpr (C_.value == 1 && u == TF) ldg2<0>(chk, vff + 262144, rsW, 0u);
        SB();
      });
    });
    lds_st<f32x4>(x1b, acc[6]);
    lds_st<f32x4>(x1b + 1024, acc[7]);
    BARRIER();
    f32x4 k0 = acc[4], k1 = acc[5];
#pragma unroll
    for (int i = 0; i < 3; ++i) {
      k0 += lds_ld<f32x4>(xw + w * 6144 + i * 2048);
      k1 += lds_ld<f32x4>(xw + w * 6144 + i * 2048 + 1024);
    }
    const auto s0 = __builtin_amdgcn_permlane16_swap(pk2(k0[0], k0[1]), pk2(k1[0], k1[1]), false, false);
    const auto s1 = __builtin_amdgcn_permlane16_swap(pk2(k0[2], k0[3]), pk2(k1[2], k1[3]), false, false);
    __builtin_amdgcn_raw_buffer_store_b128(v4u{s0[0], s1[0], s0[1], s1[1]}, rsOut, oo, 0, 16);
    if ((pp & 1) == 0 && cb == 0 && tid < 16) stg2(tag, vl8, rsW, (u32)(WS_EPOCH + 128 * (pp >> 1)));
  }
}

}

extern "C" void kernel_launch(void* const* d_in, const int* in_sizes, int n_in, void* d_out, int out_size, void* d_ws,
                              size_t ws_size, hipStream_t stream) {
  (void)in_sizes; (void)n_in; (void)out_size; (void)ws_size;
  (void)hipFuncSetAttribute((const void*)fused_block, hipFuncAttributeMaxDynamicSharedMemorySize, LDS_BYTES);
  hipLaunchKernelGGL(fused_block, dim3(256), dim3(NTHR), LDS_BYTES, stream, (const u16*)d_in[0], (const u16*)d_in[1],
                     (const u16*)d_in[2], (const u16*)d_in[4], (const u16*)d_in[5], (u16*)d_out, (unsigned char*)d_ws);
}
```
